```python
import torch

def _softmax(x, axis=-1):
    return torch.softmax(x, dim=axis)

def _gelu(x):
    return torch.nn.functional.gelu(x, approximate='tanh')

def _silu(x):
    return torch.nn.functional.silu(x)

def _sigmoid(x):
    return torch.sigmoid(x)

def _layernorm(x, axis=-1, eps=1e-05):
    m = x.mean(dim=axis, keepdim=True)
    v = x.var(dim=axis, keepdim=True, unbiased=False)
    return (x - m) / torch.sqrt(v + eps)

def _scatter_add(tgt, idx, src, axis=0):
    return tgt.clone().index_add_(axis, idx, src)

def _segment_sum(src, seg, n):
    out = torch.zeros((n,) + src.shape[1:], dtype=src.dtype)
    return out.index_add_(0, seg, src)

def _pad(x, pw):
    flat = tuple((v for pair in reversed(pw) for v in pair))
    return torch.nn.functional.pad(x, flat)
kernel_name = 'adaLN_cond__ssm_conv_s4__dense_activation_squared_relu__prompt'

def setup_inputs():

    _w = lambda a: (lambda t: t.to(torch.bfloat16) if t.is_floating_point() else t)(torch.as_tensor(a))

    def _A_setup():
        B, L, D, N = (1, 2048, 512, 128)
        A_re = -(lambda _x: torch.exp(torch.as_tensor(_x)))((lambda *s: torch.randn(*s, dtype=torch.bfloat16) * 12.8)(D, N))
        A_im = torch.pi * (lambda *s: torch.randn(*s, dtype=torch.bfloat16) * 0.1)(D, N)
        B_re = (lambda *s: torch.randn(*s, dtype=torch.bfloat16) * 0.2)(D, N)
        B_im = (lambda *s: torch.randn(*s, dtype=torch.bfloat16) * 0.2)(D, N)
        C_re = (lambda *s: torch.randn(*s, dtype=torch.bfloat16) * 0.2)(D, N)
        C_im = (lambda *s: torch.randn(*s, dtype=torch.bfloat16) * 0.2)(D, N)
        log_dt = (lambda *s: torch.randn(*s, dtype=torch.bfloat16) * 0.1)(D) * 0.5 - 4.0
        Dskip = (lambda *s: torch.randn(*s, dtype=torch.bfloat16) * 0.1)(D)
        return {'u': _w((lambda *s: torch.randn(*s, dtype=torch.bfloat16) * 0.05)(B, L, D)), 'A_re': _w(A_re), 'A_im': _w(A_im), 'B_re': _w(B_re), 'B_im': _w(B_im), 'C_re': _w(C_re), 'C_im': _w(C_im), 'log_dt': _w(log_dt), 'Dskip': _w(Dskip)}
    _A_in = _A_setup()

    def _B_setup():
        B, S, D, F = (1, 2048, 512, 1792)
        return {'x': _w((lambda *s: torch.randn(*s, dtype=torch.bfloat16) * 0.05)(B, S, D)), 'w1': _w((lambda *s: torch.randn(*s, dtype=torch.bfloat16) * 0.05)(D, F)), 'b1': _w((lambda *s: torch.randn(*s, dtype=torch.bfloat16) * 1.6)(F)), 'w2': _w((lambda *s: torch.randn(*s, dtype=torch.bfloat16) * 0.05)(F, D)), 'b2': _w((lambda *s: torch.randn(*s, dtype=torch.bfloat16) * 12.8)(D))}
    _B_in = _B_setup()
    return {'A_u': _A_in['u'], 'A_A_re': _A_in['A_re'], 'A_A_im': _A_in['A_im'], 'A_B_re': _A_in['B_re'], 'A_B_im': _A_in['B_im'], 'A_C_re': _A_in['C_re'], 'A_C_im': _A_in['C_im'], 'A_log_dt': _A_in['log_dt'], 'A_Dskip': _A_in['Dskip'], 'B_x': _B_in['x'], 'B_w1': _B_in['w1'], 'B_b1': _B_in['b1'], 'B_w2': _B_in['w2'], 'B_b2': _B_in['b2'], 'c': _w((lambda *s: torch.randn(*s, dtype=torch.bfloat16) * 0.2)(1, 512) * 8.0), 'w_mod': _w((lambda *s: torch.randn(*s, dtype=torch.bfloat16) * 0.2)(512, 6 * 512) * 512 ** (-0.5) * 8.0)}

def reference(A_u, A_A_re, A_A_im, A_B_re, A_B_im, A_C_re, A_C_im, A_log_dt, A_Dskip, B_x, B_w1, B_b1, B_w2, B_b2, c, w_mod):

    def _A_ref(u, A_re, A_im, B_re, B_im, C_re, C_im, log_dt, Dskip):
        Bsz, L, D = u.shape
        dt = torch.exp(log_dt)[:, None]
        A = A_re + 1j * A_im
        Bc = B_re + 1j * B_im
        Cc = C_re + 1j * C_im
        logdA = dt * A
        dB = dt * Bc
        l = (lambda x, dt: x.to(dt))(torch.arange(L), A_re.dtype)
        P = torch.exp(logdA[:, :, None] * l[None, None, :])
        K = torch.einsum('dn,dnl,dn->dl', Cc, P, dB).real
        L2 = 2 * L
        Kp = _pad(K, ((0, 0), (0, L)))
        up = (lambda x, *d: x.permute(*d))(u, 0, 2, 1)
        up = _pad(up, ((0, 0), (0, 0), (0, L)))
        Kf = (lambda x, axes=(-2, -1): torch.fft.rfft2(x.float(), dim=axes))(Kp[:, None, :], axes=(-2, -1))
        Uf = (lambda x, axes=(-2, -1): torch.fft.rfft2(x.float(), dim=axes))(up[:, :, None, :], axes=(-2, -1))
        Yf = Uf * Kf[None, :, :, :]
        y = (lambda x, s, axes=(-2, -1): torch.fft.irfft2(x, s=s, dim=axes).to(torch.bfloat16))(Yf, (1, L2), axes=(-2, -1))[:, :, 0, :L]
        y = (lambda x, *d: x.permute(*d))(y, 0, 2, 1) + u * Dskip[None, None, :]
        return y

    def _B_ref(x, w1, b1, w2, b2):
        h = torch.einsum('bsd,df->bsf', x, w1) + b1
        zero = torch.zeros_like(h)
        r = torch.where(h > zero, h, zero)
        h = r * r
        out = torch.einsum('bsf,fd->bsd', h, w2) + b2
        return out
    x = A_u
    mod = (_silu(c) @ w_mod).reshape(c.shape[0], 6, 1, -1)
    sa, ca, ga, sb, cb, gb = (mod[:, 0].clone(), mod[:, 1].clone(), mod[:, 2].clone(), mod[:, 3].clone(), mod[:, 4].clone(), mod[:, 5].clone())
    h = _layernorm(x, axis=-1) * (1 + ca) + sa
    x = x + ga * _A_ref(h, A_A_re, A_A_im, A_B_re, A_B_im, A_C_re, A_C_im, A_log_dt, A_Dskip)
    h = _layernorm(x, axis=-1) * (1 + cb) + sb
    x = x + gb * _B_ref(h, B_w1, B_b1, B_w2, B_b2)
    return x
```

```cpp
#include <hip/hip_runtime.h>
#include <hip/hip_bf16.h>
#include <stdint.h>

typedef unsigned short u16;
__device__ __forceinline__ float bf2f(u16 v) { return __uint_as_float(((unsigned)v) << 16); }
__device__ __forceinline__ u16 f2bf(float f) { unsigned u = __float_as_uint(f); return (u16)((u + 0x7fffu + ((u >> 16) & 1u)) >> 16); }
__device__ __forceinline__ float rbf(float f) { return bf2f(f2bf(f)); }

constexpr int L = 2048, D = 512, N = 128, F = 1792;

__global__ void k_mod(const u16* c, const u16* wmod, float* mod) {
    int j = blockIdx.x * blockDim.x + threadIdx.x;
    if (j >= 6 * D) return;
    float acc = 0.f;
    for (int k = 0; k < D; ++k) {
        float cv = bf2f(c[k]);
        float s = rbf(cv / (1.f + __expf(-cv)));
        acc += s * bf2f(wmod[k * 6 * D + j]);
    }
    mod[j] = rbf(acc);
}

__global__ void k_ln(const u16* x, const float* shift, const float* scale, u16* h) {
    int row = blockIdx.x; int lane = threadIdx.x;
    float v[8]; float s = 0.f;
    for (int i = 0; i < 8; ++i) { v[i] = bf2f(x[row * D + lane * 8 + i]); s += v[i]; }
    for (int o = 32; o; o >>= 1) s += __shfl_xor(s, o);
    float m = s / D; float q = 0.f;
    for (int i = 0; i < 8; ++i) { float d = v[i] - m; q += d * d; }
    for (int o = 32; o; o >>= 1) q += __shfl_xor(q, o);
    float var = rbf(q / D); float mb = rbf(m);
    float sd = rbf(sqrtf(rbf(var + 1e-5f)));
    for (int i = 0; i < 8; ++i) {
        int ch = lane * 8 + i;
        float xn = rbf(rbf(v[i] - mb) / sd);
        float r = rbf(rbf(xn * rbf(1.f + scale[ch])) + shift[ch]);
        h[row * D + ch] = f2bf(r);
    }
}

__global__ void k_kgen(const u16* Are, const u16* Aim, const u16* Bre, const u16* Bim, const u16* Cre, const u16* Cim,
                       const u16* logdt, float* K) {
    int d = blockIdx.y; int l = blockIdx.x * blockDim.x + threadIdx.x;
    float dt = rbf(expf(bf2f(logdt[d])));
    float lf = rbf((float)l);
    float acc = 0.f;
    for (int n = 0; n < N; ++n) {
        int i = d * N + n;
        float a = dt * bf2f(Are[i]), w = dt * bf2f(Aim[i]);
        float br = dt * bf2f(Bre[i]), bi = dt * bf2f(Bim[i]);
        float cr = bf2f(Cre[i]), ci = bf2f(Cim[i]);
        float pr = cr * br - ci * bi, pi = cr * bi + ci * br;
        float e = expf(a * lf); float sn, cs; sincosf(w * lf, &sn, &cs);
        acc += e * (pr * cs - pi * sn);
    }
    K[d * L + l] = acc;
}

__global__ void k_conv(const u16* x, const u16* h, const float* K, const u16* Dskip, const float* ga, u16* x2) {
    int d = threadIdx.x + blockIdx.x * blockDim.x; int t = blockIdx.y;
    float acc = 0.f;
    for (int s = 0; s <= t; ++s) acc += K[d * L + (t - s)] * bf2f(h[s * D + d]);
    float y = rbf(acc);
    float u = bf2f(h[t * D + d]);
    float y2 = rbf(y + rbf(u * bf2f(Dskip[d])));
    x2[t * D + d] = f2bf(bf2f(x[t * D + d]) + rbf(ga[d] * y2));
}

__global__ void k_g1(const u16* h, const u16* w1, const u16* b1, u16* hm) {
    int f = threadIdx.x + blockIdx.x * blockDim.x; int t = blockIdx.y;
    float acc = 0.f;
    for (int k = 0; k < D; ++k) acc += bf2f(h[t * D + k]) * bf2f(w1[k * F + f]);
    float v = rbf(rbf(acc) + bf2f(b1[f]));
    v = v > 0.f ? v : 0.f;
    hm[t * F + f] = f2bf(v * v);
}

__global__ void k_g2(const u16* hm, const u16* w2, const u16* b2, const u16* x2, const float* gb, u16* out) {
    int d = threadIdx.x + blockIdx.x * blockDim.x; int t = blockIdx.y;
    float acc = 0.f;
    for (int k = 0; k < F; ++k) acc += bf2f(hm[t * F + k]) * bf2f(w2[k * D + d]);
    float v = rbf(rbf(acc) + bf2f(b2[d]));
    out[t * D + d] = f2bf(bf2f(x2[t * D + d]) + rbf(gb[d] * v));
}

extern "C" void kernel_launch(void* const* d_in, const int* in_sizes, int n_in,
                              void* d_out, int out_size, void* d_ws, size_t ws_size,
                              hipStream_t stream) {
    const u16* x = (const u16*)d_in[0];
    const u16 *Are = (const u16*)d_in[1], *Aim = (const u16*)d_in[2], *Bre = (const u16*)d_in[3], *Bim = (const u16*)d_in[4];
    const u16 *Cre = (const u16*)d_in[5], *Cim = (const u16*)d_in[6], *logdt = (const u16*)d_in[7], *Dskip = (const u16*)d_in[8];
    const u16 *w1 = (const u16*)d_in[10], *b1 = (const u16*)d_in[11], *w2 = (const u16*)d_in[12], *b2 = (const u16*)d_in[13];
    const u16 *c = (const u16*)d_in[14], *wmod = (const u16*)d_in[15];
    char* ws = (char*)d_ws;
    float* mod = (float*)ws; ws += 16384;
    u16* h = (u16*)ws; ws += L * D * 2;
    u16* x2 = (u16*)ws; ws += L * D * 2;
    u16* h2 = (u16*)ws; ws += L * D * 2;
    float* K = (float*)ws; ws += D * L * 4;
    u16* hm = (u16*)ws; ws += L * F * 2;
    k_mod<<<12, 256, 0, stream>>>(c, wmod, mod);
    k_ln<<<L, 64, 0, stream>>>(x, mod + 0 * D, mod + 1 * D, h);
    k_kgen<<<dim3(L / 256, D), 256, 0, stream>>>(Are, Aim, Bre, Bim, Cre, Cim, logdt, K);
    k_conv<<<dim3(D / 256, L), 256, 0, stream>>>(x, h, K, Dskip, mod + 2 * D, x2);
    k_ln<<<L, 64, 0, stream>>>(x2, mod + 3 * D, mod + 4 * D, h2);
    k_g1<<<dim3(F / 256, L), 256, 0, stream>>>(h2, w1, b1, hm);
    k_g2<<<dim3(D / 256, L), 256, 0, stream>>>(hm, w2, b2, x2, mod + 5 * D, (u16*)d_out);
}
```

```cpp
#include <hip/hip_runtime.h>
#include <hip/hip_bf16.h>
#include <stdint.h>
#include <type_traits>

#define STAMPS 0

namespace mk {

typedef unsigned short u16;
typedef unsigned u4 __attribute__((ext_vector_type(4)));
typedef unsigned u2 __attribute__((ext_vector_type(2)));
typedef float f4 __attribute__((ext_vector_type(4)));
typedef float f2 __attribute__((ext_vector_type(2)));
typedef __bf16 bf2 __attribute__((ext_vector_type(2)));
typedef __bf16 bf8 __attribute__((ext_vector_type(8)));
typedef short s4 __attribute__((ext_vector_type(4)));
typedef short s2 __attribute__((ext_vector_type(2)));
typedef __amdgpu_buffer_rsrc_t rsrc_t;
#define LAS __attribute__((address_space(3)))
#define GAS __attribute__((address_space(1)))
#define DEV __device__ __forceinline__

constexpr int L = 2048, D = 512, NM = 128, F = 1792;
constexpr int WS_EP = 0, WS_WFLAG = 1024, WS_ST = 4096, WS_MODG = 32768, WS_XA = 131072;
constexpr int WS_XB = WS_XA + 256 * 16384, WS_XC = WS_XB + 32 * 65536, WS_W1F = WS_XC + 32 * 229376;
constexpr int WS_W2F = WS_W1F + D * F * 2, WS_END = WS_W2F + D * F * 2;
constexpr int WS_STAMP = 32 << 20;
constexpr int NSTAMP = 24;
constexpr int UV_CH = 53 * 528;
constexpr int LD_UV = 0;
constexpr int KC_CP = 4160, KC_CH = 2 * KC_CP;
constexpr int LD_KC = 0;
constexpr int LD_AF = 0;
constexpr int LD_PART = 0;
constexpr int LD_WT = 65536;
constexpr int LD_XT = 65536 + 16896;
constexpr int LD_MRED = LD_XT + 8320;
constexpr int LD_RED = 65536;
constexpr int LD_KP = 131072;
constexpr int LD_STATS = 131072;
constexpr int LD_HB = LD_KP + 4096;
constexpr int LD_XBUF = LD_HB + 9216;
constexpr int LD_XRES = LD_XBUF + 8192;
constexpr int LD_VEC = LD_XRES + 8192;
constexpr int LD_END = LD_VEC + 2048 + 256;
#define CODE_PF_DW 8400
static_assert(LD_END <= 163840, "LDS");
constexpr int AUX_SC1 = 16;
constexpr unsigned SENT = 0xFFFFFFFFu;
constexpr int SPIN_CAP = 40000;

DEV float asf(unsigned v) { return __builtin_bit_cast(float, v); }
DEV unsigned asu(float v) { return __builtin_bit_cast(unsigned, v); }
DEV float lo2f(unsigned v) { return asf(v << 16); }
DEV float hi2f(unsigned v) { return asf(v & 0xffff0000u); }
DEV unsigned pk(float a, float b) { bf2 v = {(__bf16)a, (__bf16)b}; return __builtin_bit_cast(unsigned, v); }
DEV unsigned pk(f2 a) { return pk(a.x, a.y); }
DEV float rbf(float a) { return lo2f(pk(a, a)); }
DEV f4 mfma(u4 a, u4 b, f4 c) { return __builtin_amdgcn_mfma_f32_16x16x32_bf16(__builtin_bit_cast(bf8, a), __builtin_bit_cast(bf8, b), c, 0, 0, 0); }
template <int CTRL, int RM = 0xf> DEV float dpp0(float x) { return asf((unsigned)__builtin_amdgcn_update_dpp(0, (int)asu(x), CTRL, RM, 0xf, true)); }
DEV float rdl(float x, int l) { return asf((unsigned)__builtin_amdgcn_readlane((int)asu(x), l)); }
DEV float swap32_add(float a, float b) { auto r = __builtin_amdgcn_permlane32_swap(asu(a), asu(b), false, false); return asf(r[0]) + asf(r[1]); }
DEV float swap16_add(float a, float b) { auto r = __builtin_amdgcn_permlane16_swap(asu(a), asu(b), false, false); return asf(r[0]) + asf(r[1]); }
DEV float dot2(unsigned a, unsigned b, float c) { return __builtin_amdgcn_fdot2_f32_bf16(__builtin_bit_cast(bf2, a), __builtin_bit_cast(bf2, b), c, false); }
DEV void sb0() { __builtin_amdgcn_sched_barrier(0); }
DEV void bar() { sb0(); asm volatile("s_waitcnt lgkmcnt(0)\n\ts_barrier" ::: "memory"); sb0(); }
DEV void drain() { asm volatile("s_waitcnt vmcnt(0)" ::: "memory"); }
DEV void pin() { asm volatile("" ::: "memory"); sb0(); }
DEV bool any_bad(bool bad) { return __builtin_amdgcn_ballot_w64(bad) != 0ull; }
DEV rsrc_t mkrs(const void* p, int bytes) { return __builtin_amdgcn_make_buffer_rsrc((void*)p, 0, bytes, 0x00020000); }
DEV u4 ld4(rsrc_t r, int vo, int so) { return __builtin_amdgcn_raw_buffer_load_b128(r, vo, so, 0); }
DEV u4 ld4c(rsrc_t r, int vo, int so) { return __builtin_amdgcn_raw_buffer_load_b128(r, vo, so, AUX_SC1); }
DEV void st4c(u4 v, rsrc_t r, int vo, int so) { __builtin_amdgcn_raw_buffer_store_b128(v, r, vo, so, AUX_SC1); asm volatile("s_nop 1" :: "v"(v) : "memory"); }
DEV void st2c(u2 v, rsrc_t r, int vo, int so) { __builtin_amdgcn_raw_buffer_store_b64(v, r, vo, so, AUX_SC1); }
template <class T> DEV T gld(const void* base, unsigned off) { return *(const GAS T*)((const GAS unsigned char*)base + off); }
DEV unsigned umax3(unsigned a, unsigned b, unsigned c) { return max(max(a, b), c); }
DEV f2 rot(f2 b) { return f2{-b.y, b.x}; }
DEV f2 cmul(f2 a, f2 b, f2 brot) { return __builtin_elementwise_fma(a.xx, b, a.yy * brot); }
DEV f2 zpow(float a2, float wr, float e) {
    const float m = __builtin_amdgcn_exp2f(a2 * e), ph = __builtin_amdgcn_fractf(wr * e);
    return f2{m * __builtin_amdgcn_cosf(ph), m * __builtin_amdgcn_sinf(ph)};
}
DEV int kslot(int l) {
    const int fb = (int)(pk((float)l, 0.f) & 0xffffu) - 0x4380 + 256;
    const int r = l < 128 ? l : fb;
    return l < 0 ? 641 : r;
}
DEV u4 scale4(unsigned x0, unsigned x1, unsigned x2, unsigned x3, const unsigned (&sl)[4], const unsigned (&sh)[4]) {
    unsigned o0, o1, o2, o3, t0, t1, t2, t3;
    asm("v_dot2_f32_bf16 %0, %8, %12, 0\n\tv_dot2_f32_bf16 %4, %8, %16, 0\n\tv_dot2_f32_bf16 %1, %9, %13, 0\n\tv_dot2_f32_bf16 %5, %9, %17, 0\n\t"
        "v_dot2_f32_bf16 %2, %10, %14, 0\n\tv_dot2_f32_bf16 %6, %10, %18, 0\n\tv_dot2_f32_bf16 %3, %11, %15, 0\n\tv_dot2_f32_bf16 %7, %11, %19, 0\n\t"
        "v_cvt_pk_bf16_f32 %0, %0, %4\n\tv_cvt_pk_bf16_f32 %1, %1, %5\n\tv_cvt_pk_bf16_f32 %2, %2, %6\n\tv_cvt_pk_bf16_f32 %3, %3, %7"
        : "=&v"(o0), "=&v"(o1), "=&v"(o2), "=&v"(o3), "=&v"(t0), "=&v"(t1), "=&v"(t2), "=&v"(t3)
        : "v"(x0), "v"(x1), "v"(x2), "v"(x3), "v"(sl[0]), "v"(sl[1]), "v"(sl[2]), "v"(sl[3]), "v"(sh[0]), "v"(sh[1]), "v"(sh[2]), "v"(sh[3]));
    return u4{o0, o1, o2, o3};
}

struct Params {
    const u16 *x, *Are, *Aim, *Bre, *Bim, *Cre, *Cim, *logdt, *dskip, *w1, *b1, *w2, *b2, *c, *wmod;
    u16* out;
    unsigned char* ws;
};

#define STAMP(i) do { } while (0)
#define RETRY(id, BAD, RELOAD) do { if (__builtin_expect(any_bad(BAD), 0)) { int spin_ = 0; do { asm volatile("; retry " #id ::: "memory"); __builtin_amdgcn_s_sleep(1); RELOAD; } while (any_bad(BAD) && ++spin_ < SPIN_CAP); } } while (0)

__global__ void __launch_bounds__(512, 2) fused(Params P) {
    extern __shared__ __attribute__((aligned(16))) unsigned char smem_[];
    LAS unsigned char* const sm = (LAS unsigned char*)smem_;
    STAMP(0);
    const int tid = threadIdx.x, lane = tid & 63, w = __builtin_amdgcn_readfirstlane(tid >> 6);
    const int l15 = lane & 15, lg = lane >> 4;
    const int b = blockIdx.x, idx = b >> 3, p = (b & 7) * 32 + idx, pan = p >> 3, j = p & 7, cl = idx >> 3;

    unsigned pfv;
    {
        unsigned long long pc; asm volatile("s_getpc_b64 %0" : "=s"(pc));
        const unsigned o = (unsigned)((lane & 15) * 32 + idx) * 128u;
        pfv = gld<unsigned>((const void*)(pc & ~127ull), o < CODE_PF_DW * 4u ? o : 0u);
    }
    pin();
    const rsrc_t rw = mkrs(P.ws, WS_END);
    const u4 xrow = gld<u4>(P.x, (unsigned)((8 * p + w) * D + 8 * lane) * 2u);
    unsigned ep = __builtin_amdgcn_raw_buffer_load_b32(rw, WS_EP, 0, AUX_SC1);
    const bool isw1 = p < 128, isw = p < 240;
    u4 wb0, wb1;
    {
        const rsrc_t rsw = mkrs(isw1 ? P.w1 : P.w2, D * F * 2);
        const int pitch = isw1 ? F * 2 : D * 2;
        const int base = isw1 ? (32 * (p >> 3) * F + 224 * (p & 7)) * 2 : (32 * ((p - 128) >> 1) * D + 256 * (p & 1)) * 2;
        const int ch = tid & 31;
        const int vo = (isw1 && ch >= 28) ? 0x40000000 : (tid >> 5) * pitch + ch * 16;
        wb0 = ld4(rsw, vo, base); wb1 = ld4(rsw, vo, base + 16 * pitch);
    }
    const bool ismod = p >= 64;
    const int mcb = (p - 64) >> 2, mq = p & 3;
    u4 wm0, wm1; unsigned rc0, rc1;
    {
        const rsrc_t rsm = mkrs(P.wmod, ismod ? 6 * D * D * 2 : 0);
        const int r = 128 * mq + 16 * w + (lane >> 3);
        const int vo = (r * (6 * D) + 8 * (lane & 7)) * 2, so = ismod ? 128 * mcb : 0;
        wm0 = ld4(rsm, vo, so); wm1 = ld4(rsm, vo, so + 8 * 6 * D * 2);
        rc0 = gld<u16>(P.c, r * 2); rc1 = gld<u16>(P.c, r * 2 + 16);
    }
    const int kch = (w >> 1) & 1, kd = 2 * p + kch;
    const unsigned ko = (unsigned)(kd * NM + (w & 1) * 64 + lane) * 2u;
    unsigned rdt = gld<u16>(P.logdt, kd * 2), rAre = gld<u16>(P.Are, ko), rAim = gld<u16>(P.Aim, ko);
    unsigned rBre = gld<u16>(P.Bre, ko), rBim = gld<u16>(P.Bim, ko), rCre = gld<u16>(P.Cre, ko), rCim = gld<u16>(P.Cim, ko);
    pin();
    if (w == 7) {
        const u4 s = {SENT, SENT, SENT, SENT};
#pragma unroll
        for (int i = 0; i < 8; ++i) st4c(s, rw, lane * 16 + i * 1024, WS_XB + p * 8192);
    }
    pin();
    int ks[9];
    {
        const int q = tid & 255;
#pragma unroll
        for (int i = 0; i < 9; ++i) ks[i] = LD_KP + (tid >> 8) * 1312 + 2 * kslot(2047 - 8 * q - i);
#pragma unroll
        for (int i = 0; i < 9; ++i) asm volatile("" : "+v"(ks[i]));
    }
    if (tid < 64) *(LAS u4*)(sm + LD_HB + (tid >> 5) * 4608 + (tid & 31) * 16) = u4{0, 0, 0, 0};
    if (tid < 2) *(LAS u16*)(sm + LD_KP + tid * 1312 + 641 * 2) = 0;
    pin();

    *(LAS u4*)(sm + LD_XT + w * 1040 + lane * 16) = xrow;
    STAMP(1);
    unsigned tag;
    {
        float v[8] = {lo2f(xrow.x), hi2f(xrow.x), lo2f(xrow.y), hi2f(xrow.y), lo2f(xrow.z), hi2f(xrow.z), lo2f(xrow.w), hi2f(xrow.w)};
        const float sh = asf(__builtin_amdgcn_readfirstlane(asu(v[0])));
        float s = 0.f, q = 0.f;
#pragma unroll
        for (int i = 0; i < 8; ++i) { const float d = v[i] - sh; s += d; q += d * d; }
        float t = swap32_add(s, q);
        t += dpp0<0xB1>(t); t += dpp0<0x4E>(t); t += dpp0<0x141>(t); t += dpp0<0x140>(t); t += dpp0<0x142, 0xa>(t);
        const float md = rdl(t, 31) * (1.f / D), var = rbf(fmaxf(rdl(t, 63) * (1.f / D) - md * md, 0.f));
        const float sd = __builtin_amdgcn_sqrtf(rbf(var + 1e-5f));
        tag = __builtin_amdgcn_readfirstlane(ep) * 0x9E3779B1u + 0x7F4A7C15u;
        if (w == 7) drain();
        if (lane == 0) st2c(u2{pk(md + sh, sd), tag}, rw, 0, WS_ST + (8 * p + w) * 8);
    }
    pin();
    *(LAS u4*)(sm + LD_WT + (tid >> 5) * 528 + (tid & 31) * 16) = wb0;
    *(LAS u4*)(sm + LD_WT + (16 + (tid >> 5)) * 528 + (tid & 31) * 16) = wb1;
    {
        const float c0 = lo2f(rc0), c1 = lo2f(rc1);
        const float s0 = rbf(c0 * __builtin_amdgcn_rcpf(1.f + __expf(-c0))), s1 = rbf(c1 * __builtin_amdgcn_rcpf(1.f + __expf(-c1)));
        float a[8];
#pragma unroll
        for (int i = 0; i < 4; ++i) { a[2 * i] = s0 * lo2f(wm0[i]) + s1 * lo2f(wm1[i]); a[2 * i + 1] = s0 * hi2f(wm0[i]) + s1 * hi2f(wm1[i]); }
        const float e0 = swap32_add(a[0], a[4]), e1 = swap32_add(a[1], a[5]), e2 = swap32_add(a[2], a[6]), e3 = swap32_add(a[3], a[7]);
        float g0 = swap16_add(e0, e2), g1 = swap16_add(e1, e3);
        g0 += dpp0<0x128>(g0); g1 += dpp0<0x128>(g1);
        if (l15 < 8) *(LAS f2*)(sm + LD_MRED + w * 256 + (8 * l15 + 2 * lg) * 4) = f2{g0, g1};
    }
    STAMP(2);
    bar();
    {
        const int pp = tid >> 2, q = tid & 3;
        u4 g[2];
#pragma unroll
        for (int i = 0; i < 2; ++i) g[i] = u4{*(LAS unsigned*)(sm + LD_XT + (2 * q) * 1040 + (pp + 128 * i) * 4), tag, *(LAS unsigned*)(sm + LD_XT + (2 * q + 1) * 1040 + (pp + 128 * i) * 4), tag};
        asm volatile("" : "+v"(g[0]), "+v"(g[1]));
#pragma unroll
        for (int i = 0; i < 2; ++i) st4c(g[i], rw, pp * 16384 + q * 16, WS_XA + i * 128 * 16384 + p * 64);
    }
    if (ismod && w == 0) {
        float s = 0.f;
#pragma unroll
        for (int i = 0; i < 8; ++i) s += *(LAS float*)(sm + LD_MRED + i * 256 + lane * 4);
        st2c(u2{asu(s), tag}, rw, lane * 32, WS_MODG + mcb * 2048 + mq * 8);
    }
    {
        const int cbase = isw1 ? 32 * w : 64 * (w >> 1) + 8 * (w & 1), cs = isw1 ? 8 : 16;
        const int a = LD_WT + (8 * lg + (l15 >> 2)) * 528 + (cbase + cs * (lane & 3)) * 2;
        int dst = isw1 ? WS_W1F + (7 * (p & 7) + w) * 32768 + (p >> 3) * 2048
                       : WS_W2F + (4 * (p & 1) + (w >> 1)) * 229376 + ((p - 128) >> 1) * 4096 + (w & 1) * 2048;
        if (!isw || (isw1 && w == 7)) dst = 0x40000000;
#pragma unroll
        for (int e = 0; e < 2; ++e) {
            const u2 t0 = __builtin_bit_cast(u2, __builtin_amdgcn_ds_read_tr16_b64_v4i16((LAS s4*)(sm + a + e * 8)));
            const u2 t1 = __builtin_bit_cast(u2, __builtin_amdgcn_ds_read_tr16_b64_v4i16((LAS s4*)(sm + a + e * 8 + 4 * 528)));
            st4c(u4{t0.x, t0.y, t1.x, t1.y}, rw, lane * 16 + e * 1024, dst);
        }
    }
    pin();
    STAMP(3);

    {
        const float dt = rbf(expf(lo2f(rdt)));
        const float a2 = (dt * lo2f(rAre)) * 1.44269504088896341f, wr = (dt * lo2f(rAim)) * 0.15915494309189535f;
        LAS unsigned char* t = sm + LD_UV + kch * UV_CH + ((w & 1) * 64 + lane) * 4;
        if (w < 4) {
            f2 q[8];
            q[1] = zpow(a2, wr, 8.f); q[2] = zpow(a2, wr, 16.f); q[4] = zpow(a2, wr, 32.f);
            const f2 h1 = zpow(a2, wr, 64.f), h2 = zpow(a2, wr, 128.f);
            const f2 h1r = rot(h1), h2r = rot(h2), q4r = rot(q[4]);
            const f2 h3 = cmul(h1, h2, h2r), h3r = rot(h3);
            q[3] = cmul(q[1], q[2], rot(q[2])); q[5] = cmul(q[1], q[4], q4r); q[6] = cmul(q[2], q[4], q4r); q[7] = cmul(q[3], q[4], q4r);
            *(LAS unsigned*)(t + 21 * 528) = 0x00003f80u;
            *(LAS unsigned*)(t + 29 * 528) = pk(h1); *(LAS unsigned*)(t + 37 * 528) = pk(h2); *(LAS unsigned*)(t + 45 * 528) = pk(h3);
#pragma unroll
            for (int i = 1; i < 8; ++i) {
                *(LAS unsigned*)(t + (21 + i) * 528) = pk(q[i]);
                *(LAS unsigned*)(t + (29 + i) * 528) = pk(cmul(q[i], h1, h1r));
                *(LAS unsigned*)(t + (37 + i) * 528) = pk(cmul(q[i], h2, h2r));
                *(LAS unsigned*)(t + (45 + i) * 528) = pk(cmul(q[i], h3, h3r));
            }
        } else {
            const f2 bc = {dt * lo2f(rBre), -(dt * lo2f(rBim))};
            const f2 cf = cmul(f2{lo2f(rCre), -lo2f(rCim)}, bc, rot(bc));
            const f2 z1 = zpow(a2, -wr, 1.f), z2 = zpow(a2, -wr, 2.f), z4 = zpow(a2, -wr, 4.f);
            const f2 s1 = zpow(a2, -wr, 256.f), s2_ = zpow(a2, -wr, 512.f), s4_ = zpow(a2, -wr, 1024.f);
            const f2 z2r = rot(z2), z4r = rot(z4), s1r = rot(s1), s2r = rot(s2_), s4r = rot(s4_);
            f2 u[21];
            u[0] = cf; u[1] = cmul(cf, z1, rot(z1)); u[2] = cmul(cf, z2, z2r); u[4] = cmul(cf, z4, z4r);
            u[3] = cmul(u[1], z2, z2r); u[5] = cmul(u[1], z4, z4r); u[6] = cmul(u[2], z4, z4r); u[7] = cmul(u[3], z4, z4r);
            u[8] = cmul(u[0], s1, s1r); u[9] = cmul(u[2], s1, s1r); u[10] = cmul(u[4], s1, s1r); u[11] = cmul(u[6], s1, s1r);
            u[12] = cmul(u[0], s2_, s2r); u[13] = cmul(u[4], s2_, s2r); u[14] = cmul(u[8], s2_, s2r); u[15] = cmul(u[10], s2_, s2r);
            u[16] = cmul(u[0], s4_, s4r); u[17] = cmul(u[8], s4_, s4r); u[18] = cmul(u[12], s4_, s4r); u[19] = cmul(u[14], s4_, s4r); u[20] = cmul(u[16], s4_, s4r);
#pragma unroll
            for (int i = 0; i < 21; ++i) *(LAS unsigned*)(t + i * 528) = pk(u[i]);
        }
    }
    STAMP(4);
    bar();
    {
        const int ch = w >> 2, rt = (w >> 1) & 1, ct = w & 1;
        LAS unsigned char* ua = sm + LD_UV + ch * UV_CH + (16 * rt + l15) * 528 + lg * 16;
        LAS unsigned char* va = sm + LD_UV + ch * UV_CH + (21 + 16 * ct + l15) * 528 + lg * 16;
        u4 fu[8], fv[8];
#pragma unroll
        for (int s = 0; s < 8; ++s) { fu[s] = *(LAS u4*)(ua + s * 64); fv[s] = *(LAS u4*)(va + s * 64); }
        sb0();
        f4 k0 = {0, 0, 0, 0};
#pragma unroll
        for (int s = 0; s < 8; ++s) k0 = mfma(fu[s], fv[s], k0);
        const int col = 16 * ct + l15;
        const unsigned v01 = pk(k0[0], k0[1]), v23 = pk(k0[2], k0[3]);
        LAS unsigned char* kp = sm + LD_KP + ch * 1312;
        if (rt == 0) {
            const int p0 = lg < 2 ? 8 * col + 4 * lg : (lg == 2 ? 256 + 4 * col : 384 + 2 * col);
            const int p2 = lg == 3 ? p0 + 64 : p0 + 2;
            *(LAS unsigned*)(kp + p0 * 2) = v01; *(LAS unsigned*)(kp + p2 * 2) = v23;
        } else {
            if (lg == 0) {
                *(LAS u16*)(kp + (512 + col) * 2) = (u16)v01; *(LAS u16*)(kp + (544 + col) * 2) = (u16)(v01 >> 16);
                *(LAS u16*)(kp + (576 + col) * 2) = (u16)v23; *(LAS u16*)(kp + (608 + col) * 2) = (u16)(v23 >> 16);
            } else if (lg == 1 && col == 0) *(LAS u16*)(kp + 640 * 2) = (u16)v01;
        }
    }
    STAMP(5);
    drain();
    STAMP(6);
    asm volatile("" :: "v"(pfv));
    bar();
    if (tid == 0) st2c(u2{tag, ~tag}, rw, 0, WS_WFLAG + p * 8);
    u4 xa[2], st[2], mg;
    const int go = tid * 16 + w * 1024, mo = lane < 12 ? (lane >> 2) * 512 * 32 + (lane & 3) * 16 : 0;
    auto ldA = [&]() {
        xa[0] = ld4c(rw, go, WS_XA + p * 16384); xa[1] = ld4c(rw, go + 1024, WS_XA + p * 16384);
        st[0] = ld4c(rw, go, WS_ST); st[1] = ld4c(rw, go + 1024, WS_ST);
        mg = ld4c(rw, mo, WS_MODG + p * 64);
    };
    ldA();
    pin();
    {
        unsigned v[9];
#pragma unroll
        for (int i = 0; i < 9; ++i) v[i] = *(LAS u16*)(sm + ks[i]);
        LAS unsigned char* kc = sm + LD_KC + (tid >> 8) * KC_CH + (tid & 255) * 16;
        *(LAS u4*)kc = u4{v[0] | (v[1] << 16), v[2] | (v[3] << 16), v[4] | (v[5] << 16), v[6] | (v[7] << 16)};
        *(LAS u4*)(kc + KC_CP) = u4{v[1] | (v[2] << 16), v[3] | (v[4] << 16), v[5] | (v[6] << 16), v[7] | (v[8] << 16)};
        if (tid < 8) *(LAS u4*)(sm + LD_KC + (tid >> 1) * KC_CP + 4096 + (tid & 1) * 16) = u4{0, 0, 0, 0};
    }
    bar();
    u4 A[16];
    {
        const int ch = w >> 2, wq = w & 3, bp = 15 - l15;
        LAS unsigned char* ab = sm + LD_KC + ch * KC_CH + (bp & 1) * KC_CP + (bp >> 1) * 4 + (24 + lg - 8 * wq) * 16;
#pragma unroll
        for (int n = 0; n < 16; ++n) { LAS unsigned* ap = (LAS unsigned*)(ab + (228 - 4 * ((n >> 3) + 8 * (n & 7))) * 16); A[n] = u4{ap[0], ap[1], ap[2], ap[3]}; }
    }
    pin();
    STAMP(7);
    RETRY(1, ((xa[0].y ^ tag) | (xa[0].w ^ tag) | (xa[1].y ^ tag) | (xa[1].w ^ tag) | (st[0].y ^ tag) | (st[0].w ^ tag) | (st[1].y ^ tag) | (st[1].w ^ tag) | (mg.y ^ tag) | (mg.w ^ tag)) != 0, ldA());
    STAMP(8);
    u4 fl0, fl1;
    auto ldF = [&]() { fl0 = ld4c(rw, lane * 16, WS_WFLAG); fl1 = ld4c(rw, lane * 16 + 1024, WS_WFLAG); };
    ldF();
    const unsigned rds = gld<unsigned>(P.dskip, 4 * p);
    pin();
    float ga0, ga1;
    {
        float mv = asf(mg.x) + asf(mg.z);
        mv = rbf(mv + dpp0<0xB1>(mv));
        const float sa0 = rdl(mv, 0), sa1 = rdl(mv, 2), c0 = rbf(1.f + rdl(mv, 4)), c1 = rbf(1.f + rdl(mv, 6));
        ga0 = rdl(mv, 8); ga1 = rdl(mv, 10);
#pragma unroll
        for (int i = 0; i < 2; ++i) {
            const int t = 256 * w + 128 * i + 2 * lane;
            const float m0 = lo2f(st[i].x), r0 = __builtin_amdgcn_rcpf(hi2f(st[i].x)), m1 = lo2f(st[i].z), r1 = __builtin_amdgcn_rcpf(hi2f(st[i].z));
            const float h00 = (lo2f(xa[i].x) - m0) * r0 * c0 + sa0, h01 = (lo2f(xa[i].z) - m1) * r1 * c0 + sa0;
            const float h10 = (hi2f(xa[i].x) - m0) * r0 * c1 + sa1, h11 = (hi2f(xa[i].z) - m1) * r1 * c1 + sa1;
            *(LAS unsigned*)(sm + LD_HB + (256 + t) * 2) = pk(h00, h01);
            *(LAS unsigned*)(sm + LD_HB + 4608 + (256 + t) * 2) = pk(h10, h11);
            *(LAS u2*)(sm + LD_XBUF + t * 4) = u2{xa[i].x, xa[i].z};
        }
    }
    bar();
    if (b == 0 && tid == 0) __builtin_amdgcn_raw_buffer_store_b32(tag, rw, WS_EP, 0, AUX_SC1);
    STAMP(9);

    f4 acc[8];
    u4 B1[8];
    {
        LAS unsigned char* bb = sm + LD_HB + (w >> 2) * 4608 + (208 - 64 * (w & 3) + 16 * l15 + 8 * lg) * 2;
        u4 B0[8];
#pragma unroll
        for (int k = 0; k < 8; ++k) B0[k] = *(LAS u4*)(bb + (256 * k + 32) * 2);
        sb0();
#pragma unroll
        for (int k = 0; k < 8; ++k) {
            B1[k] = *(LAS u4*)(bb + (256 * k) * 2);
#pragma unroll
            for (int i = 0; i + k < 8; ++i) acc[i + k] = mfma(A[i], B0[k], k == 0 ? f4{0, 0, 0, 0} : acc[i + k]);
            sb0();
        }
    }
    STAMP(10);
    RETRY(2, ((fl0.x ^ tag) | (fl0.z ^ tag) | (fl1.x ^ tag) | (fl1.z ^ tag) | (~fl0.y ^ tag) | (~fl0.w ^ tag) | (~fl1.y ^ tag) | (~fl1.w ^ tag)) != 0, ldF());
    STAMP(11);

    u4 wf[32];
    const int w1base = WS_W1F + (7 * j + w) * 32768;
    auto ldw1 = [&](int n) { wf[n] = ld4c(rw, lane * 16, w1base + (((n >> 1) + 4 * cl) & 15) * 2048 + (n & 1) * 1024); };
    u4 mv4[4], mv5;
    constexpr int NW1A = 10;
    auto conv2 = [&](auto W7c) __attribute__((always_inline)) {
        constexpr bool W7 = decltype(W7c)::value;
#pragma unroll
        for (int k = 0; k < 8; ++k) {
            if (k < 4) mv4[k] = ld4c(rw, tid * 16 + k * 8192, WS_MODG + 1536 * 32);
            if (k == 0) mv5 = ld4c(rw, (tid & 127) * 16, WS_MODG + (2560 + 64 * j) * 32);
#pragma unroll
            for (int i = 0; i + k < 8; ++i) {
                acc[i + k] = mfma(A[8 + i], B1[k], acc[i + k]);
                const int n = (k * (17 - k)) / 2 + i;
                if (n % 3 == 2) {
                    if (!W7) { if (n / 3 < NW1A) ldw1(n / 3); }
                    else if (n / 3 < 7) {
#pragma unroll
                        for (int q = 0; q < 4; ++q) st4c(u4{SENT, SENT, SENT, SENT}, rw, lane * 16 + q * 1024, WS_XC + p * 28672 + (n / 3) * 4096);
                    }
                    sb0();
                }
            }
            sb0();
        }
        LAS unsigned char* rd = sm + LD_RED + w * 8192 + (4 * l15 + (lg ^ ((l15 >> 1) & 3))) * 16;
#pragma unroll
        for (int c = 0; c < 8; ++c) *(LAS f4*)(rd + c * 1024) = acc[c];
    };
    if (w == 7) conv2(std::true_type{}); else conv2(std::false_type{});
    sb0();
    {
        float s[4];
#pragma unroll
        for (int i = 0; i < 4; ++i) { s[i] = asf(mv4[i].x) + asf(mv4[i].z); s[i] = rbf(s[i] + dpp0<0xB1>(s[i])); if (i >= 2) s[i] += 1.f; }
        const int d = ((tid & 1) * 256 + (tid >> 1) - 128 * cl) & 511;
        const unsigned v = pk((tid & 1) ? s[3] : s[2], (tid & 1) ? s[1] : s[0]);
        *(LAS u16*)(sm + LD_VEC + d * 2) = (u16)v; *(LAS u16*)(sm + LD_VEC + 1024 + d * 2) = (u16)(v >> 16);
        float g = asf(mv5.x) + asf(mv5.z);
        g = rbf(g + dpp0<0xB1>(g));
        *(LAS float*)(sm + LD_VEC + 2048 + ((tid & 127) >> 1) * 4) = g;
    }
    if (w == 7) drain();
    STAMP(12);
    bar();
    {
        LAS unsigned char* rd = sm + LD_RED + w * 1024 + ((lane & ~3) + ((lane & 3) ^ ((lane >> 3) & 3))) * 16;
        f4 y0 = (*(LAS f4*)(rd) + *(LAS f4*)(rd + 8192)) + (*(LAS f4*)(rd + 2 * 8192) + *(LAS f4*)(rd + 3 * 8192));
        f4 y1 = (*(LAS f4*)(rd + 4 * 8192) + *(LAS f4*)(rd + 5 * 8192)) + (*(LAS f4*)(rd + 6 * 8192) + *(LAS f4*)(rd + 7 * 8192));
        const int t = 256 * w + 4 * lane;
        const u2 h0 = *(LAS u2*)(sm + LD_HB + (256 + t) * 2), h1 = *(LAS u2*)(sm + LD_HB + 4608 + (256 + t) * 2);
        const u4 xv = *(LAS u4*)(sm + LD_XBUF + t * 4);
        const float d0 = lo2f(rds), d1 = hi2f(rds);
        const float u0[4] = {lo2f(h0.x), hi2f(h0.x), lo2f(h0.y), hi2f(h0.y)}, u1[4] = {lo2f(h1.x), hi2f(h1.x), lo2f(h1.y), hi2f(h1.y)};
        u4 o;
#pragma unroll
        for (int r = 0; r < 4; ++r) o[r] = min(pk(lo2f(xv[r]) + ga0 * (y0[r] + u0[r] * d0), hi2f(xv[r]) + ga1 * (y1[r] + u1[r] * d1)), SENT - 1u);
        st4c(o, rw, lg * 65536 + l15 * 16, WS_XB + 4 * w * 65536 + p * 256);
    }
    pin();
    STAMP(13);
    auto rest = [&](auto W7c) __attribute__((always_inline)) {
    constexpr bool W7 = decltype(W7c)::value;
    u4 rb1;
    if (!W7) {
        rb1 = gld<u4>(P.b1, (224 * j + 32 * w + 8 * lg) * 2);
#pragma unroll
        for (int n = NW1A; n < 32; ++n) ldw1(n);
    }
    const int pg = tid >> 4;
    u4 xb[8];
    auto ldB = [&]() {
#pragma unroll
        for (int i = 0; i < 8; ++i) xb[i] = ld4c(rw, pg * 2048 + l15 * 16 + i * 256, WS_XB + pan * 65536);
    };
    auto badB = [&]() {
        unsigned mx = 0;
#pragma unroll
        for (int i = 0; i < 8; ++i) mx = umax3(umax3(mx, xb[i].x, xb[i].y), xb[i].z, xb[i].w);
        return mx == SENT;
    };
    ldB();
    pin();
    f4 cs[2], bs[2];
    if (!W7) {
        f4 q0 = {0, 0, 0, 0}, q1 = {0, 0, 0, 0};
#pragma unroll
        for (int h = 0; h < 4; ++h) {
            u4 vb[4];
#pragma unroll
            for (int s = 0; s < 4; ++s) vb[s] = *(LAS u4*)(sm + LD_VEC + (lane & 1) * 1024 + (4 * h + s) * 64 + lg * 16);
#pragma unroll
            for (int s = 0; s < 4; ++s) { q0 = mfma(wf[8 * h + 2 * s], vb[s], q0); q1 = mfma(wf[8 * h + 2 * s + 1], vb[s], q1); }
        }
#pragma unroll
        for (int r = 0; r < 4; ++r) {
            cs[0][r] = dpp0<0xA0>(q0[r]); cs[1][r] = dpp0<0xA0>(q1[r]);
            bs[0][r] = dpp0<0xF5>(q0[r]) + ((r & 1) ? hi2f(rb1[r >> 1]) : lo2f(rb1[r >> 1]));
            bs[1][r] = dpp0<0xF5>(q1[r]) + ((r & 1) ? hi2f(rb1[2 + (r >> 1)]) : lo2f(rb1[2 + (r >> 1)]));
        }
        asm volatile("" : "+v"(cs[0]), "+v"(cs[1]), "+v"(bs[0]), "+v"(bs[1]));
    }
    unsigned sl[2][4], sh[2][4];
    {
        const u4 sv0 = *(LAS u4*)(sm + LD_VEC + ((16 * pg - 128 * cl) & 511) * 2), sv1 = *(LAS u4*)(sm + LD_VEC + ((16 * pg - 128 * cl) & 511) * 2 + 16);
#pragma unroll
        for (int i = 0; i < 4; ++i) { sl[0][i] = sv0[i] & 0xffffu; sh[0][i] = sv0[i] & 0xffff0000u; sl[1][i] = sv1[i] & 0xffffu; sh[1][i] = sv1[i] & 0xffff0000u; }
    }
    pin();
    STAMP(14);

    RETRY(3, badB(), ldB());
    STAMP(15);
    {
        LAS unsigned char* af = sm + LD_AF + (((pg >> 1) - 4 * cl) & 15) * 1024 + (2 * (pg & 1) * 16 + l15) * 16;
#pragma unroll
        for (int h = 0; h < 2; ++h)
#pragma unroll
            for (int r = 0; r < 4; ++r) *(LAS u4*)(af + r * 16384 + h * 256) = scale4(xb[4 * h][r], xb[4 * h + 1][r], xb[4 * h + 2][r], xb[4 * h + 3][r], sl[h], sh[h]);
    }
    bar();
    STAMP(16);
    const int w2base = WS_W2F + j * 229376 + w * 28672;
    const int rot7 = 2 * cl;
    auto ldw2 = [&](int n) { int i = (n >> 2) + rot7; i = i >= 7 ? i - 7 : i; wf[n] = ld4c(rw, lane * 16, w2base + i * 4096 + (n & 3) * 1024); };
    if (!W7) {
        f4 ac[2][4];
        u4 hb[2][4];
#pragma unroll
        for (int r = 0; r < 4; ++r) hb[0][r] = *(LAS u4*)(sm + LD_AF + r * 16384 + lane * 16);
        sb0();
#pragma unroll
        for (int s = 0; s < 16; ++s) {
#pragma unroll
            for (int r = 0; r < 4; ++r) {
                if (s < 15) hb[(s + 1) & 1][r] = *(LAS u4*)(sm + LD_AF + r * 16384 + (s + 1) * 1024 + lane * 16);
                if (s >= 1 && s < 15 && (r & 1)) ldw2(2 * (s - 1) + (r >> 1));
                ac[0][r] = mfma(wf[2 * s], hb[s & 1][r], s == 0 ? f4{0, 0, 0, 0} : ac[0][r]);
                ac[1][r] = mfma(wf[2 * s + 1], hb[s & 1][r], s == 0 ? f4{0, 0, 0, 0} : ac[1][r]);
                sb0();
            }
        }
        STAMP(17);
        bar();
        const f4 st0 = *(LAS f4*)(sm + LD_STATS + l15 * 32), st1 = *(LAS f4*)(sm + LD_STATS + l15 * 32 + 16);
        const float rs[4] = {st0[0], st0[2], st1[0], st1[2]}, nm[4] = {st0[1], st0[3], st1[1], st1[3]};
#pragma unroll
        for (int r = 0; r < 4; ++r) {
            u4 o;
#pragma unroll
            for (int q = 0; q < 4; ++q) {
                const int e = q >> 1, c = (q & 1) * 2;
                const float v0 = (ac[e][r][c] + nm[r] * cs[e][c]) * rs[r] + bs[e][c], v1 = (ac[e][r][c + 1] + nm[r] * cs[e][c + 1]) * rs[r] + bs[e][c + 1];
                const unsigned h = pk(v0 * __builtin_fabsf(v0), v1 * __builtin_fabsf(v1));
                o[q] = __builtin_bit_cast(unsigned, __builtin_elementwise_max(__builtin_bit_cast(s2, h), s2{0, 0}));
            }
            st4c(o, rw, lane * 16 + r * 1024, WS_XC + pan * 229376 + (7 * j + w) * 4096);
            sb0();
        }
    } else {
        float sum[4] = {0, 0, 0, 0}, sq[4] = {0, 0, 0, 0};
        u4 y[2][8];
#pragma unroll
        for (int i = 0; i < 8; ++i) y[0][i] = ld4c(rw, lg * 2048 + l15 * 16 + i * 256, WS_XB + pan * 65536 + j * 8192);
#pragma unroll
        for (int g = 0; g < 8; ++g) {
            if (g < 7) {
#pragma unroll
                for (int i = 0; i < 8; ++i) y[(g + 1) & 1][i] = ld4c(rw, lg * 2048 + l15 * 16 + i * 256, WS_XB + pan * 65536 + ((j + g + 1) & 7) * 8192);
            }
            sb0();
            if (g == 0) {
#pragma unroll
                for (int r = 0; r < 4; ++r) {
                    LAS unsigned char* xr = sm + LD_XRES + (4 * l15 + r) * 128 + lg * 32;
                    *(LAS u4*)xr = u4{y[0][0][r], y[0][1][r], y[0][2][r], y[0][3][r]}; *(LAS u4*)(xr + 16) = u4{y[0][4][r], y[0][5][r], y[0][6][r], y[0][7][r]};
                }
            }
#pragma unroll
            for (int i = 0; i < 8; ++i)
#pragma unroll
                for (int r = 0; r < 4; ++r) { sum[r] = dot2(y[g & 1][i][r], 0x3f803f80u, sum[r]); sq[r] = dot2(y[g & 1][i][r], y[g & 1][i][r], sq[r]); }
            sb0();
        }
#pragma unroll
        for (int n = 0; n < 28; ++n) ldw2(n);
        const float e0 = swap32_add(sum[0], sum[2]), e1 = swap32_add(sum[1], sum[3]), e2 = swap32_add(sq[0], sq[2]), e3 = swap32_add(sq[1], sq[3]);
        const float m = swap16_add(e0, e1) * (1.f / D), var = rbf(fmaxf(swap16_add(e2, e3) * (1.f / D) - m * m, 0.f));
        const float rs = __builtin_amdgcn_rcpf(rbf(__builtin_amdgcn_sqrtf(rbf(var + 1e-5f))));
        *(LAS f2*)(sm + LD_STATS + (l15 * 4 + lg) * 8) = f2{rs, -rbf(m)};
        STAMP(17);
        bar();
    }
    const u4 rb2 = gld<u4>(P.b2, (64 * j + 8 * (lane & 7)) * 2);
    pin();
    STAMP(18);

    {
        int ko7[7];
#pragma unroll
        for (int i = 0; i < 7; ++i) { int k = i + rot7; k = k >= 7 ? k - 7 : k; ko7[i] = WS_XC + pan * 229376 + (7 * w + k) * 4096; }
        u4 hm[3][7];
        auto ldg = [&](u4 (&h)[7], int rt) {
#pragma unroll
            for (int i = 0; i < 7; ++i) h[i] = ld4c(rw, lane * 16 + ((rt + j) & 3) * 1024, ko7[i]);
        };
        auto badg = [&](u4 (&h)[7]) {
            unsigned o = 0;
#pragma unroll
            for (int i = 0; i < 7; ++i) o |= (h[i].x | h[i].y) | (h[i].z | h[i].w);
            return (o & 0x80008000u) != 0;
        };
        ldg(hm[0], 0);
        RETRY(4, badg(hm[0]), ldg(hm[0], 0));
        STAMP(19);
        ldg(hm[1], 1); ldg(hm[2], 2);
        pin();
#pragma unroll
        for (int rt = 0; rt < 4; ++rt) {
            if (rt > 0) RETRY(5, badg(hm[rt % 3]), ldg(hm[rt % 3], rt));
            f4 ac[4];
#pragma unroll
            for (int i = 0; i < 7; ++i)
#pragma unroll
                for (int m = 0; m < 4; ++m) ac[m] = mfma(wf[4 * i + m], hm[rt % 3][i], i == 0 ? f4{0, 0, 0, 0} : ac[m]);
            sb0();
            if (rt == 0) ldg(hm[0], 3);
            LAS unsigned char* pp = sm + LD_PART + w * 16384 + ((rt + j) & 3) * 4096 + lg * 256;
#pragma unroll
            for (int m = 0; m < 4; ++m) *(LAS f4*)(pp + m * 1024 + ((l15 + 4 * lg + 8 * (m >> 1)) & 15) * 16) = ac[m];
            pin();
        }
    }
    STAMP(20);
    bar();
    {
        const int rt = w >> 1, tp = 8 * (w & 1) + (lane >> 3), ck = lane & 7, g = ck >> 1;
        LAS unsigned char* pp = sm + LD_PART + rt * 4096 + (ck & 1) * 2048 + (g * 16 + ((tp + 4 * g + 8 * (ck & 1)) & 15)) * 16;
        f4 s0 = {0, 0, 0, 0}, s1 = {0, 0, 0, 0};
#pragma unroll
        for (int i = 0; i < 8; ++i) { s0 += *(LAS f4*)(pp + i * 16384); s1 += *(LAS f4*)(pp + i * 16384 + 1024); }
        const int row = 4 * tp + rt;
        const u4 xr = *(LAS u4*)(sm + LD_XRES + row * 128 + ck * 16);
        const f4 g0 = *(LAS f4*)(sm + LD_VEC + 2048 + ck * 32), g1 = *(LAS f4*)(sm + LD_VEC + 2048 + ck * 32 + 16);
        u4 o;
        o.x = pk(lo2f(xr.x) + g0[0] * (s0[0] + lo2f(rb2.x)), hi2f(xr.x) + g0[1] * (s0[1] + hi2f(rb2.x)));
        o.y = pk(lo2f(xr.y) + g0[2] * (s0[2] + lo2f(rb2.y)), hi2f(xr.y) + g0[3] * (s0[3] + hi2f(rb2.y)));
        o.z = pk(lo2f(xr.z) + g1[0] * (s1[0] + lo2f(rb2.z)), hi2f(xr.z) + g1[1] * (s1[1] + hi2f(rb2.z)));
        o.w = pk(lo2f(xr.w) + g1[2] * (s1[2] + lo2f(rb2.w)), hi2f(xr.w) + g1[3] * (s1[3] + hi2f(rb2.w)));
        *(GAS u4*)((GAS unsigned char*)P.out + (unsigned)((64 * pan + row) * D + 64 * j + 8 * ck) * 2u) = o;
    }
    };
    if (w == 7) rest(std::true_type{}); else rest(std::false_type{});
}

}

extern "C" void kernel_launch(void* const* d_in, const int* in_sizes, int n_in, void* d_out, int out_size, void* d_ws, size_t ws_size, hipStream_t stream) {
    using namespace mk;
    Params P;
    P.x = (const u16*)d_in[0]; P.Are = (const u16*)d_in[1]; P.Aim = (const u16*)d_in[2]; P.Bre = (const u16*)d_in[3]; P.Bim = (const u16*)d_in[4];
    P.Cre = (const u16*)d_in[5]; P.Cim = (const u16*)d_in[6]; P.logdt = (const u16*)d_in[7]; P.dskip = (const u16*)d_in[8];
    P.w1 = (const u16*)d_in[10]; P.b1 = (const u16*)d_in[11]; P.w2 = (const u16*)d_in[12]; P.b2 = (const u16*)d_in[13];
    P.c = (const u16*)d_in[14]; P.wmod = (const u16*)d_in[15];
    P.out = (u16*)d_out; P.ws = (unsigned char*)d_ws;
    (void)hipFuncSetAttribute((const void*)fused, hipFuncAttributeMaxDynamicSharedMemorySize, LD_END);
    hipLaunchKernelGGL(fused, dim3(256), dim3(512), LD_END, stream, P);
}
```

```cpp
#include <hip/hip_runtime.h>
#include <hip/hip_bf16.h>
#include <stdint.h>
#include <type_traits>

#define EXP 0
#define STAMPS 0

namespace mk {

typedef unsigned short u16;
typedef unsigned u4 __attribute__((ext_vector_type(4)));
typedef unsigned u2 __attribute__((ext_vector_type(2)));
typedef float f4 __attribute__((ext_vector_type(4)));
typedef float f2 __attribute__((ext_vector_type(2)));
typedef __bf16 bf2 __attribute__((ext_vector_type(2)));
typedef __bf16 bf8 __attribute__((ext_vector_type(8)));
typedef short s4 __attribute__((ext_vector_type(4)));
typedef short s2 __attribute__((ext_vector_type(2)));
typedef __amdgpu_buffer_rsrc_t rsrc_t;
#define LAS __attribute__((address_space(3)))
#define GAS __attribute__((address_space(1)))
#define DEV __device__ __forceinline__

constexpr int L = 2048, D = 512, NM = 128, F = 1792;
constexpr int WS_EP = 0, WS_M2 = 8192, WS_ST = 16384, WS_WFLAG = 32768, WS_MODG = 65536, WS_FLC = 196608;
constexpr int XA_STR = 16384 + 4096 + 256 + 128, XB_STR = 65536 + 4096 + 256, XC_STR = 229376;
constexpr int WS_XA = WS_FLC + 32 * 64 * 128, WS_XB = WS_XA + 256 * XA_STR, WS_XC = WS_XB + 32 * XB_STR, WS_W1F = WS_XC + 32 * XC_STR;
constexpr int WS_W2F = WS_W1F + D * F * 2, WS_END = WS_W2F + D * F * 2;
constexpr int WS_STAMP = 32 << 20;
constexpr int NSTAMP = 24;
constexpr int UV_CH = 63 * 528;
constexpr int LD_UV = 0;
constexpr int LD_RED = 0;
constexpr int LD_AF = 0;
constexpr int LD_PART = 0;
constexpr int KC_CP = 4160, KC_CH = 2 * KC_CP;
constexpr int LD_KC = 66560;
constexpr int LD_WT = LD_KC + 16640;
constexpr int LD_XT = LD_WT + 16384;
constexpr int LD_MRED = LD_XT + 9216;
constexpr int LD_STATP = LD_KC;
constexpr int LD_STATS = 131072;
constexpr int LD_HB = LD_STATS + 512;
constexpr int LD_XBUF = LD_HB + 9216;
constexpr int LD_XRES = LD_XBUF + 8192;
constexpr int LD_VEC = LD_XRES + 8192;
constexpr int LD_CNT = LD_VEC + 2048 + 256;
constexpr int LD_END = LD_CNT + 16;
static_assert(LD_MRED + 2048 <= 131072, "LDS");
#define CODE_PF_DW 5300
static_assert(LD_END <= 163840, "LDS");
constexpr int AUX_SC1 = 16;
constexpr unsigned SENT = 0xFFFFFFFFu;
constexpr int SPIN_CAP = 40000;

DEV float asf(unsigned v) { return __builtin_bit_cast(float, v); }
DEV unsigned asu(float v) { return __builtin_bit_cast(unsigned, v); }
DEV float lo2f(unsigned v) { return asf(v << 16); }
DEV float hi2f(unsigned v) { return asf(v & 0xffff0000u); }
DEV unsigned pk(float a, float b) { bf2 v = {(__bf16)a, (__bf16)b}; return __builtin_bit_cast(unsigned, v); }
DEV unsigned pk(f2 a) { return pk(a.x, a.y); }
DEV float rbf(float a) { return lo2f(pk(a, a)); }
DEV f4 mfma(u4 a, u4 b, f4 c) { return __builtin_amdgcn_mfma_f32_16x16x32_bf16(__builtin_bit_cast(bf8, a), __builtin_bit_cast(bf8, b), c, 0, 0, 0); }
template <int CTRL, int RM = 0xf> DEV float dpp0(float x) { return asf((unsigned)__builtin_amdgcn_update_dpp(0, (int)asu(x), CTRL, RM, 0xf, true)); }
DEV float rdl(float x, int l) { return asf((unsigned)__builtin_amdgcn_readlane((int)asu(x), l)); }
DEV float swap32_add(float a, float b) { auto r = __builtin_amdgcn_permlane32_swap(asu(a), asu(b), false, false); return asf(r[0]) + asf(r[1]); }
DEV float swap16_add(float a, float b) { auto r = __builtin_amdgcn_permlane16_swap(asu(a), asu(b), false, false); return asf(r[0]) + asf(r[1]); }
DEV float dot2(unsigned a, unsigned b, float c) { return __builtin_amdgcn_fdot2_f32_bf16(__builtin_bit_cast(bf2, a), __builtin_bit_cast(bf2, b), c, false); }
DEV void sb0() { __builtin_amdgcn_sched_barrier(0); }
DEV void bar() { sb0(); asm volatile("s_waitcnt lgkmcnt(0)\n\ts_barrier" ::: "memory"); sb0(); }
DEV void drain() { asm volatile("s_waitcnt vmcnt(0)" ::: "memory"); }
DEV void pin() { asm volatile("" ::: "memory"); sb0(); }
DEV int opq(int v) { asm volatile("" : "+s"(v)); return v; }
DEV bool any_bad(bool bad) { return __builtin_amdgcn_ballot_w64(bad) != 0ull; }
DEV rsrc_t mkrs(const void* p, int bytes) { return __builtin_amdgcn_make_buffer_rsrc((void*)p, 0, bytes, 0x00020000); }
DEV u4 ld4(rsrc_t r, int vo, int so) { return __builtin_amdgcn_raw_buffer_load_b128(r, vo, so, 0); }
DEV u4 ld4c(rsrc_t r, int vo, int so) { return __builtin_amdgcn_raw_buffer_load_b128(r, vo, so, AUX_SC1); }
DEV void st4c(u4 v, rsrc_t r, int vo, int so) { __builtin_amdgcn_raw_buffer_store_b128(v, r, vo, so, AUX_SC1); asm volatile("s_nop 1" :: "v"(v) : "memory"); }
DEV u2 ld2c(rsrc_t r, int vo, int so) { return __builtin_amdgcn_raw_buffer_load_b64(r, vo, so, AUX_SC1); }
DEV void st2c(u2 v, rsrc_t r, int vo, int so) { __builtin_amdgcn_raw_buffer_store_b64(v, r, vo, so, AUX_SC1); }
template <class T> DEV T gld(const void* base, unsigned off) { return *(const GAS T*)((const GAS unsigned char*)base + off); }
DEV unsigned umax3(unsigned a, unsigned b, unsigned c) { return max(max(a, b), c); }
DEV f2 rot(f2 b) { return f2{-b.y, b.x}; }
DEV f2 cmul(f2 a, f2 b, f2 brot) { return __builtin_elementwise_fma(a.xx, b, a.yy * brot); }
DEV f2 csq(f2 a, f2 arot) { return cmul(a, a, arot); }
DEV f2 zpow(float a2, float wr, float e) {
    const float m = __builtin_amdgcn_exp2f(a2 * e), ph = __builtin_amdgcn_fractf(wr * e);
    return f2{m * __builtin_amdgcn_cosf(ph), m * __builtin_amdgcn_sinf(ph)};
}
DEV int urow(int blk, int r) {
    const int lb = (int)lo2f(pk((float)(256 * blk + r), 0.f)) - 256 * blk;
    return blk == 0 ? r : (blk == 1 ? 16 + (lb >> 1) : (blk < 4 ? 15 + 5 * blk + (lb >> 2) : 23 + 3 * blk + (lb >> 3)));
}
DEV u4 scale4(unsigned x0, unsigned x1, unsigned x2, unsigned x3, const unsigned (&sl)[4], const unsigned (&sh)[4]) {
    unsigned o0, o1, o2, o3, t0, t1, t2, t3;
    asm("v_dot2_f32_bf16 %0, %8, %12, 0\n\tv_dot2_f32_bf16 %4, %8, %16, 0\n\tv_dot2_f32_bf16 %1, %9, %13, 0\n\tv_dot2_f32_bf16 %5, %9, %17, 0\n\t"
        "v_dot2_f32_bf16 %2, %10, %14, 0\n\tv_dot2_f32_bf16 %6, %10, %18, 0\n\tv_dot2_f32_bf16 %3, %11, %15, 0\n\tv_dot2_f32_bf16 %7, %11, %19, 0\n\t"
        "v_cvt_pk_bf16_f32 %0, %0, %4\n\tv_cvt_pk_bf16_f32 %1, %1, %5\n\tv_cvt_pk_bf16_f32 %2, %2, %6\n\tv_cvt_pk_bf16_f32 %3, %3, %7"
        : "=&v"(o0), "=&v"(o1), "=&v"(o2), "=&v"(o3), "=&v"(t0), "=&v"(t1), "=&v"(t2), "=&v"(t3)
        : "v"(x0), "v"(x1), "v"(x2), "v"(x3), "v"(sl[0]), "v"(sl[1]), "v"(sl[2]), "v"(sl[3]), "v"(sh[0]), "v"(sh[1]), "v"(sh[2]), "v"(sh[3]));
    return u4{o0, o1, o2, o3};
}

struct Params {
    const u16 *x, *Are, *Aim, *Bre, *Bim, *Cre, *Cim, *logdt, *dskip, *w1, *b1, *w2, *b2, *c, *wmod;
    u16* out;
    unsigned char* ws;
};

#define STAMP(i) do { } while (0)
#define RETRY(id, BAD, RELOAD) do { if (__builtin_expect(any_bad(BAD), 0)) { int spin_ = 0; do { asm volatile("; retry " #id ::: "memory"); __builtin_amdgcn_s_sleep(2); RELOAD; } while (any_bad(BAD) && ++spin_ < SPIN_CAP); } } while (0)

__global__ void __launch_bounds__(512, 2) fused(Params P) {
    extern __shared__ __attribute__((aligned(16))) unsigned char smem_[];
    LAS unsigned char* const sm = (LAS unsigned char*)smem_;
    STAMP(0);
    const int tid = threadIdx.x, lane = tid & 63, w = __builtin_amdgcn_readfirstlane(tid >> 6);
    const int l15 = lane & 15, lg = lane >> 4;
    const int b = blockIdx.x, g8 = b & 7, idx = b >> 3, p = g8 * 32 + idx, cl = idx >> 3;
    const int pan1 = 8 * (g8 >> 1) + (idx >> 2), j1 = 4 * (g8 & 1) + (idx & 3);
    const int pan2 = p >> 3, j2 = p & 7;

    const rsrc_t rw = mkrs(P.ws, WS_END);
    if (w == 7) {
#pragma unroll
        for (int i = 0; i < 8; ++i) st4c(u4{SENT, SENT, SENT, SENT}, rw, lane * 16 + i * 1024, WS_XB + (p >> 3) * XB_STR + (p & 7) * 8192);
    }
    pin();
    const bool isw1 = w < 4;
    const int wB = 4 * b + (w & 3), wS = wB / 56, wF = wB - 56 * wS;
    u4 wb0, wb1;
    {
        const rsrc_t rsw = mkrs(isw1 ? P.w1 : P.w2, D * F * 2);
        const int pitch = isw1 ? F * 2 : D * 2;
        const int base = isw1 ? (32 * wS * F + 32 * wF) * 2 : (32 * (wB >> 4) * D + 32 * (wB & 15)) * 2;
        const int vo = (lane >> 2) * pitch + (lane & 3) * 16;
        wb0 = ld4(rsw, vo, base); wb1 = ld4(rsw, vo, base + 16 * pitch);
    }
    const int kch = (w >> 1) & 1, kd = 2 * p + kch;
    const unsigned ko = (unsigned)(kd * NM + (w & 1) * 64 + lane) * 2u;
    unsigned rdt = gld<u16>(P.logdt, kd * 2), rAre = gld<u16>(P.Are, ko), rAim = gld<u16>(P.Aim, ko);
    unsigned rBre = gld<u16>(P.Bre, ko), rBim = gld<u16>(P.Bim, ko), rCre = gld<u16>(P.Cre, ko), rCim = gld<u16>(P.Cim, ko);
    const bool ismod = b >= 64;
    const int mcb = (b - 64) >> 2, mq = b & 3;
    u4 wm0, wm1; unsigned rc0, rc1;
    {
        const rsrc_t rsm = mkrs(P.wmod, ismod ? 6 * D * D * 2 : 0);
        const int r = 128 * mq + 16 * w + (lane >> 3);
        const int vo = (r * (6 * D) + 8 * (lane & 7)) * 2, so = ismod ? 128 * mcb : 0;
        wm0 = ld4(rsm, vo, so); wm1 = ld4(rsm, vo, so + 8 * 6 * D * 2);
        rc0 = gld<u16>(P.c, r * 2); rc1 = gld<u16>(P.c, r * 2 + 16);
    }
    const u4 xblk = gld<u4>(P.x, (unsigned)((8 * p + (lane >> 3)) * D + 64 * w + 8 * (lane & 7)) * 2u);
    const u4 xrow = gld<u4>(P.x, (unsigned)((8 * p + w) * D + 8 * lane) * 2u);
    unsigned ep = __builtin_amdgcn_raw_buffer_load_b32(rw, WS_EP, 0, AUX_SC1);
    unsigned pfv;
    {
        unsigned long long pc; asm volatile("s_getpc_b64 %0" : "=s"(pc));
        const unsigned o = (unsigned)((2 * w + (lane & 1)) * 32 + idx) * 128u;
        pfv = gld<unsigned>((const void*)(pc & ~127ull), o < CODE_PF_DW * 4u ? o : 0u);
    }
    pin();
    int ur0, ur1;
    {
        ur0 = LD_UV + (w >> 2) * UV_CH + urow(w & 3, 15 - l15) * 528 + lg * 16; ur1 = LD_UV + (w >> 2) * UV_CH + urow(4 + (w & 3), 15 - l15) * 528 + lg * 16;
        asm volatile("" : "+v"(ur0), "+v"(ur1));
    }
    if (tid < 64) *(LAS u4*)(sm + LD_HB + (tid >> 5) * 4608 + (tid & 31) * 16) = u4{0, 0, 0, 0};
    if (tid == 64) *(LAS unsigned*)(sm + LD_CNT) = 0u;
    if (tid < 16) *(LAS u4*)(sm + LD_KC + (tid >> 2) * KC_CP + 4080 + (tid & 3) * 16) = u4{0, 0, 0, 0};
    pin();

    {
        LAS unsigned char* wt = sm + LD_WT + w * 2048;
        *(LAS u4*)(wt + lane * 16) = wb0; *(LAS u4*)(wt + 1024 + lane * 16) = wb1;
        STAMP(1);
        LAS unsigned char* a = wt + (8 * lg + (l15 >> 2)) * 64 + (lane & 3) * 16;
        int dst = isw1 ? WS_W1F + wF * 32768 + wS * 2048 : WS_W2F + ((wB & 15) >> 1) * 229376 + (wB >> 4) * 4096 + (wB & 1) * 2048;
        if (b >= 224) dst = 0x40000000;
#pragma unroll
        for (int e = 0; e < 2; ++e) {
            const u2 t0 = __builtin_bit_cast(u2, __builtin_amdgcn_ds_read_tr16_b64_v4i16((LAS s4*)(a + e * 8)));
            const u2 t1 = __builtin_bit_cast(u2, __builtin_amdgcn_ds_read_tr16_b64_v4i16((LAS s4*)(a + e * 8 + 4 * 64)));
            st4c(u4{t0.x, t0.y, t1.x, t1.y}, rw, lane * 16 + e * 1024, dst);
        }
    }
    pin();
    STAMP(2);
    {
        const float dt = rbf(expf(lo2f(rdt)));
        const float a2 = (dt * lo2f(rAre)) * 1.44269504088896341f, wr = (dt * lo2f(rAim)) * 0.15915494309189535f;
        const f2 bc = {dt * lo2f(rBre), dt * lo2f(rBim)};
        const f2 cf = cmul(f2{lo2f(rCre), lo2f(rCim)}, bc, rot(bc));
        const f2 z2 = zpow(a2, wr, 2.f), z2r = rot(z2), z4 = csq(z2, z2r), z4r = rot(z4), z8 = csq(z4, z4r), z8r = rot(z8);
        const f2 zz = zpow(a2, wr, w < 4 ? 1.f : 256.f), zzr = rot(zz);
        LAS unsigned char* t = sm + LD_UV + kch * UV_CH + ((w & 1) * 64 + lane) * 4;
        auto put = [&](int row, f2 v) { *(LAS unsigned*)(t + row * 528) = pk(v); };
        auto putc = [&](int row, f2 v) { *(LAS unsigned*)(t + row * 528) = pk(v.x, -v.y); };
        if (w < 4) {
            f2 u[8], v[8];
            v[1] = csq(z8, z8r); v[2] = csq(v[1], rot(v[1]));
            const f2 y2r = rot(v[2]); v[4] = csq(v[2], y2r);
            const f2 y4r = rot(v[4]), y8 = csq(v[4], y4r), y8r = rot(y8);
            u[0] = cf; u[1] = cmul(cf, zz, zzr); u[2] = cmul(u[0], z2, z2r); u[3] = cmul(u[1], z2, z2r);
#pragma unroll
            for (int i = 0; i < 4; ++i) u[4 + i] = cmul(u[i], z4, z4r);
#pragma unroll
            for (int i = 0; i < 8; ++i) { put(i, u[i]); put(8 + i, cmul(u[i], z8, z8r)); }
            v[3] = cmul(v[1], v[2], y2r);
#pragma unroll
            for (int i = 1; i < 4; ++i) v[4 + i] = cmul(v[i], v[4], y4r);
            *(LAS unsigned*)(t + 47 * 528) = 0x00003f80u; putc(55, y8);
#pragma unroll
            for (int i = 1; i < 8; ++i) { putc(47 + i, v[i]); putc(55 + i, cmul(v[i], y8, y8r)); }
        } else {
            const f2 s2_ = csq(zz, zzr), s2r = rot(s2_), s4_ = csq(s2_, s2r), s4r = rot(s4_);
            f2 e[9], c2[5], c3[5];
            e[0] = cmul(cf, zz, zzr); e[1] = cmul(e[0], z2, z2r); e[2] = cmul(e[0], z4, z4r); e[3] = cmul(e[1], z4, z4r);
#pragma unroll
            for (int i = 0; i < 5; ++i) e[4 + i] = cmul(e[i], z8, z8r);
#pragma unroll
            for (int i = 0; i < 9; ++i) put(16 + i, e[i]);
#pragma unroll
            for (int i = 0; i < 5; ++i) { c2[i] = cmul(e[2 * i], zz, zzr); c3[i] = cmul(e[2 * i], s2_, s2r); put(25 + i, c2[i]); put(30 + i, c3[i]); }
#pragma unroll
            for (int i = 0; i < 3; ++i) { put(35 + i, cmul(c2[2 * i], s2_, s2r)); put(38 + i, cmul(e[4 * i], s4_, s4r)); put(41 + i, cmul(c2[2 * i], s4_, s4r)); put(44 + i, cmul(c3[2 * i], s4_, s4r)); }
        }
    }
    pin();
    STAMP(3);
    drain();
    asm volatile("" :: "v"(pfv));
    STAMP(4);
    const unsigned tag = __builtin_amdgcn_readfirstlane(ep) * 0x9E3779B1u + 0x7F4A7C15u;
    st4c(u4{~tag, tag, ~tag, tag}, rw, (lane == 0 && b < 224) ? 0 : 0x40000000, WS_WFLAG + (isw1 ? wF * 256 + wS * 16 : 14336 + ((wB & 15) >> 1) * 1792 + (wB >> 4) * 32 + (wB & 1) * 16));
    {
        *(LAS u4*)(sm + LD_XT + w * 1152 + (lane >> 3) * 144 + (lane & 7) * 16) = xblk;
        LAS unsigned char* xt = sm + LD_XT + w * 1152 + (lane & 3) * 288 + (lane >> 2) * 4;
        u4 g[2];
#pragma unroll
        for (int i = 0; i < 2; ++i) g[i] = u4{*(LAS unsigned*)(xt + 64 * i), tag, *(LAS unsigned*)(xt + 144 + 64 * i), tag};
#pragma unroll
        for (int i = 0; i < 2; ++i) st4c(g[i], rw, lane * 16 + i * 1024, WS_XA + p * XA_STR + w * 2048);
    }
    {
        const float c0 = lo2f(rc0), c1 = lo2f(rc1);
        const float s0 = rbf(c0 * __builtin_amdgcn_rcpf(1.f + __expf(-c0))), s1 = rbf(c1 * __builtin_amdgcn_rcpf(1.f + __expf(-c1)));
        float a[8];
#pragma unroll
        for (int i = 0; i < 4; ++i) { a[2 * i] = s0 * lo2f(wm0[i]) + s1 * lo2f(wm1[i]); a[2 * i + 1] = s0 * hi2f(wm0[i]) + s1 * hi2f(wm1[i]); }
        const float e0 = swap32_add(a[0], a[4]), e1 = swap32_add(a[1], a[5]), e2 = swap32_add(a[2], a[6]), e3 = swap32_add(a[3], a[7]);
        float g0 = swap16_add(e0, e2), g1 = swap16_add(e1, e3);
        g0 += dpp0<0x128>(g0); g1 += dpp0<0x128>(g1);
        *(LAS f2*)(sm + LD_MRED + w * 256 + (8 * (l15 & 7) + 2 * lg) * 4) = f2{g0, g1};
        float v[8] = {lo2f(xrow.x), hi2f(xrow.x), lo2f(xrow.y), hi2f(xrow.y), lo2f(xrow.z), hi2f(xrow.z), lo2f(xrow.w), hi2f(xrow.w)};
        const float sh = asf(__builtin_amdgcn_readfirstlane(asu(v[0])));
        float s = 0.f, q = 0.f;
#pragma unroll
        for (int i = 0; i < 8; ++i) { const float d = v[i] - sh; s += d; q += d * d; }
        float t = swap32_add(s, q);
        t += dpp0<0xB1>(t); t += dpp0<0x4E>(t); t += dpp0<0x141>(t); t += dpp0<0x140>(t); t += dpp0<0x142, 0xa>(t);
        const float md = rdl(t, 31) * (1.f / D), var = rbf(fmaxf(rdl(t, 63) * (1.f / D) - md * md, 0.f));
        *(LAS unsigned*)(sm + LD_MRED + 2048 + w * 4) = pk(md + sh, __builtin_amdgcn_sqrtf(rbf(var + 1e-5f)));
    }
    STAMP(5);
    bar();
    if (w == 0) {
        f2 s = {0.f, 0.f};
#pragma unroll
        for (int i = 0; i < 8; ++i) s += *(LAS f2*)(sm + LD_MRED + i * 256 + (lane & 31) * 8);
        st4c(u4{asu(s.x), tag, asu(s.y), tag}, rw, (ismod && lane < 32) ? lane * 64 : 0x40000000, WS_MODG + mcb * 2048 + mq * 16);
    }
    if (w == 1) {
        const u2 sv = *(LAS u2*)(sm + LD_MRED + 2048 + (lane & 3) * 8);
        st4c(u4{sv.x, tag, sv.y, tag}, rw, lane < 4 ? lane * 16 : 0x40000000, WS_ST + p * 64);
    }
    {
        LAS unsigned char* va = sm + LD_UV + (w >> 2) * UV_CH + (47 + l15) * 528 + lg * 16;
        u4 fu[2][8], fv[8];
#pragma unroll
        for (int s = 0; s < 8; ++s) { fv[s] = *(LAS u4*)(va + s * 64); fu[0][s] = *(LAS u4*)(sm + ur0 + s * 64); fu[1][s] = *(LAS u4*)(sm + ur1 + s * 64); }
        sb0();
        f4 k[2] = {{0, 0, 0, 0}, {0, 0, 0, 0}};
#pragma unroll
        for (int s = 0; s < 8; ++s) { k[0] = mfma(fu[0][s], fv[s], k[0]); k[1] = mfma(fu[1][s], fv[s], k[1]); }
        LAS unsigned char* kc = sm + LD_KC + (w >> 2) * KC_CH + (1008 - 256 * (w & 3) - 16 * l15 + 4 * lg) * 2;
#pragma unroll
        for (int h = 0; h < 2; ++h) {
            const unsigned v01 = pk(k[h][0], k[h][1]), v23 = pk(k[h][2], k[h][3]), v12 = __builtin_amdgcn_alignbit(v23, v01, 16);
            LAS unsigned char* q = kc + (1 - h) * 2048;
            *(LAS u2*)q = u2{v01, v23};
            *(LAS u16*)(q + KC_CP - 2) = (u16)v01; *(LAS unsigned*)(q + KC_CP) = v12; *(LAS u16*)(q + KC_CP + 4) = (u16)(v23 >> 16);
        }
    }
    pin();
    u4 xa[2], st[2], mg;
    const int xo = (32 * w + (lane >> 2)) * XA_STR + (lane & 3) * 16, so_ = tid * 16 + w * 1024;
    const int mo = lane < 24 ? WS_MODG + ((lane >> 2) * 256 + p) * 64 + (lane & 3) * 16
                             : WS_WFLAG + (w < 7 ? (7 * j1 + w) * 256 + l15 * 16 : 14336 + j2 * 1792 + 49 * 32 + min(l15, 13) * 16);
    auto ldA = [&]() {
        xa[0] = ld4c(rw, xo, WS_XA + p * 64); xa[1] = ld4c(rw, xo, WS_XA + p * 64 + 16 * XA_STR);
        st[0] = ld4c(rw, so_, WS_ST); st[1] = ld4c(rw, so_ + 1024, WS_ST);
        mg = ld4c(rw, mo, 0);
    };
    const unsigned rds = gld<unsigned>(P.dskip, 4 * p);
    if (!(EXP & 1)) ldA();
    STAMP(6);
    bar();
    u4 A[16];
    LAS unsigned char* const ab = sm + LD_KC + (w >> 2) * KC_CH + ((15 - l15) & 1) * KC_CP + ((15 - l15) >> 1) * 4 + (24 + lg - 8 * (w & 3)) * 16;
    auto ldT = [&](int n) { LAS unsigned* ap = (LAS unsigned*)(ab + (228 - 4 * ((n >> 3) + 8 * (n & 7))) * 16); A[n] = u4{ap[0], ap[1], ap[2], ap[3]}; };
#pragma unroll
    for (int n = 0; n < 8; ++n) ldT(n);
    if (EXP & 1) ldA();
    pin();
    STAMP(7);
    RETRY(1, ((xa[0].y ^ tag) | (xa[0].w ^ tag) | (xa[1].y ^ tag) | (xa[1].w ^ tag) | (st[0].y ^ tag) | (st[0].w ^ tag) | (st[1].y ^ tag) | (st[1].w ^ tag) | (mg.y ^ tag) | (mg.w ^ tag)) != 0, ldA());
    STAMP(8);
    u4 wf[32];
    const int rot7 = 3 * (cl & 1);
    const int w2base = WS_W2F + j2 * 229376 + w * 28672;
    const int wsb = w < 7 ? WS_W1F + (7 * j1 + w) * 32768 : w2base, wsr = w < 7 ? 8192 * cl : 4096 * rot7, wsm = w < 7 ? 32768 : 28672;
    auto ldw1 = [&](int n) { int o = n * 1024 + wsr; o = o >= wsm ? o - wsm : o; wf[n] = ld4c(rw, lane * 16, wsb + o); };
    constexpr int NW0 = 4, NW1 = NW0 + 8, NW2 = NW1 + 8;
    float ga0, ga1;
    {
        float m0_ = asf(mg.x), m1_ = asf(mg.z);
        m0_ += dpp0<0xB1>(m0_); m1_ += dpp0<0xB1>(m1_); m0_ += dpp0<0x4E>(m0_); m1_ += dpp0<0x4E>(m1_);
        const unsigned mp = pk(m0_, m1_);
        const unsigned sap = __builtin_amdgcn_readlane(mp, 0), cap = __builtin_amdgcn_readlane(mp, 4), gap = __builtin_amdgcn_readlane(mp, 8);
        const float sa0 = lo2f(sap), sa1 = hi2f(sap), c0 = rbf(1.f + lo2f(cap)), c1 = rbf(1.f + hi2f(cap));
        ga0 = lo2f(gap); ga1 = hi2f(gap);
        if (w == 0) {
            const unsigned sbp = __builtin_amdgcn_readlane(mp, 12), cbp = __builtin_amdgcn_readlane(mp, 16), gbp = __builtin_amdgcn_readlane(mp, 20);
            const unsigned s1p = pk(1.f + lo2f(cbp), 1.f + hi2f(cbp));
            st4c(u4{lane ? gbp : sbp, tag, lane ? gbp : s1p, tag}, rw, lane < 2 ? lane * 4096 : 0x40000000, WS_M2 + p * 16);
        }
#pragma unroll
        for (int i = 0; i < 2; ++i) {
            const int t = 256 * w + 128 * i + 2 * lane;
            const float m0 = lo2f(st[i].x), r0 = __builtin_amdgcn_rcpf(hi2f(st[i].x)), m1 = lo2f(st[i].z), r1 = __builtin_amdgcn_rcpf(hi2f(st[i].z));
            const float h00 = (lo2f(xa[i].x) - m0) * r0 * c0 + sa0, h01 = (lo2f(xa[i].z) - m1) * r1 * c0 + sa0;
            const float h10 = (hi2f(xa[i].x) - m0) * r0 * c1 + sa1, h11 = (hi2f(xa[i].z) - m1) * r1 * c1 + sa1;
            *(LAS unsigned*)(sm + LD_HB + (256 + t) * 2) = pk(h00, h01);
            *(LAS unsigned*)(sm + LD_HB + 4608 + (256 + t) * 2) = pk(h10, h11);
            *(LAS u2*)(sm + LD_XBUF + t * 4) = u2{xa[i].x, xa[i].z};
        }
    }
    pin();
#pragma unroll
    for (int n = 0; n < NW0; ++n) ldw1(n);
    bar();
    if (b == 0 && tid == 0) __builtin_amdgcn_raw_buffer_store_b32(tag, rw, WS_EP, 0, AUX_SC1);
    STAMP(9);

    u4 mv;
    auto ldM = [&]() { mv = ld4c(rw, w < 4 ? tid * 16 : 4096 + j2 * 512 + (lane & 31) * 16, WS_M2); };
    {
        f4 acc[8];
        LAS unsigned char* bb = sm + LD_HB + (w >> 2) * 4608 + (208 - 64 * (w & 3) + 16 * l15 + 8 * lg) * 2;
        u4 B0[8], B1[8];
#pragma unroll
        for (int k = 0; k < 8; ++k) B0[k] = *(LAS u4*)(bb + (256 * k + 32) * 2);
        sb0();
#pragma unroll
        for (int k = 0; k < 8; ++k) {
#pragma unroll
            for (int i = 0; i + k < 8; ++i) {
                acc[i + k] = mfma(A[i], B0[k], k == 0 ? f4{0, 0, 0, 0} : acc[i + k]);
                const int n = (k * (17 - k)) / 2 + i;
                if (n % 4 == 1 && n / 4 < 8) { ldT(8 + n / 4); B1[n / 4] = *(LAS u4*)(bb + (256 * (n / 4)) * 2); sb0(); }
                if (n % 4 == 3 && NW0 + n / 4 < NW1) { ldw1(NW0 + n / 4); sb0(); }
            }
        }
        sb0();
        STAMP(10);
#pragma unroll
        for (int k = 0; k < 8; ++k) {
#pragma unroll
            for (int i = 0; i + k < 8; ++i) {
                acc[i + k] = mfma(A[8 + i], B1[k], acc[i + k]);
                const int n = (k * (17 - k)) / 2 + i;
                if (n == 33) { ldM(); sb0(); }
                if (n % 4 == 3 && NW1 + n / 4 < NW2) { ldw1(NW1 + n / 4); sb0(); }
            }
            if (k > 0) *(LAS f4*)(sm + LD_RED + w * 8192 + (4 * l15 + (lg ^ ((l15 >> 1) & 3))) * 16 + (k - 1) * 1024) = acc[k - 1];
            sb0();
        }
        *(LAS f4*)(sm + LD_RED + w * 8192 + (4 * l15 + (lg ^ ((l15 >> 1) & 3))) * 16 + 7 * 1024) = acc[7];
    }
    STAMP(12);
    bar();
    {
        LAS unsigned char* rd = sm + LD_RED + w * 1024 + ((lane & ~3) + ((lane & 3) ^ ((lane >> 3) & 3))) * 16;
        f4 y0 = (*(LAS f4*)(rd) + *(LAS f4*)(rd + 8192)) + (*(LAS f4*)(rd + 2 * 8192) + *(LAS f4*)(rd + 3 * 8192));
        f4 y1 = (*(LAS f4*)(rd + 4 * 8192) + *(LAS f4*)(rd + 5 * 8192)) + (*(LAS f4*)(rd + 6 * 8192) + *(LAS f4*)(rd + 7 * 8192));
        const int t = 256 * w + 4 * lane;
        const u2 h0 = *(LAS u2*)(sm + LD_HB + (256 + t) * 2), h1 = *(LAS u2*)(sm + LD_HB + 4608 + (256 + t) * 2);
        const u4 xv = *(LAS u4*)(sm + LD_XBUF + t * 4);
        const float d0 = lo2f(rds), d1 = hi2f(rds);
        const float u0[4] = {lo2f(h0.x), hi2f(h0.x), lo2f(h0.y), hi2f(h0.y)}, u1[4] = {lo2f(h1.x), hi2f(h1.x), lo2f(h1.y), hi2f(h1.y)};
        u4 o;
#pragma unroll
        for (int r = 0; r < 4; ++r) o[r] = min(pk(lo2f(xv[r]) + ga0 * (y0[r] + u0[r] * d0), hi2f(xv[r]) + ga1 * (y1[r] + u1[r] * d1)), SENT - 1u);
        st4c(o, rw, lg * XB_STR + l15 * 16, WS_XB + 4 * w * XB_STR + p * 256);
    }
    pin();
    STAMP(13);
    RETRY(2, ((mv.y ^ tag) | (mv.w ^ tag)) != 0, ldM());
    if (w < 4) {
        *(LAS unsigned*)(sm + LD_VEC + ((2 * tid - 128 * cl) & 511) * 2) = mv.z; *(LAS unsigned*)(sm + LD_VEC + 1024 + ((2 * tid - 128 * cl) & 511) * 2) = mv.x;
    } else *(LAS f2*)(sm + LD_VEC + 2048 + (lane & 31) * 8) = f2{lo2f(mv.x), hi2f(mv.x)};
    bar();
    STAMP(11);
    const bool W7 = w == 7;
    const u4 rb1 = gld<u4>(P.b1, (224 * j1 + 32 * (W7 ? 0 : w) + 8 * lg) * 2);
#pragma unroll
    for (int n = NW2; n < 32; ++n) ldw1(n);
    const int pg = tid >> 4;
    u4 xb[8];
    auto ldB = [&]() {
#pragma unroll
        for (int i = 0; i < 8; ++i) xb[i] = ld4c(rw, pg * 2048 + l15 * 16 + i * 256, WS_XB + pan1 * XB_STR);
    };
    auto badB = [&]() {
        unsigned mx = 0;
#pragma unroll
        for (int i = 0; i < 8; ++i) mx = umax3(umax3(mx, xb[i].x, xb[i].y), xb[i].z, xb[i].w);
        return mx == SENT;
    };
    ldB();
    pin();
    f4 cs[2], bs[2];
    if (opq(w) != 7) {
        f4 q0 = {0, 0, 0, 0}, q1 = {0, 0, 0, 0};
#pragma unroll
        for (int h = 0; h < 4; ++h) {
            u4 vb[4];
#pragma unroll
            for (int s = 0; s < 4; ++s) vb[s] = *(LAS u4*)(sm + LD_VEC + (lane & 1) * 1024 + (4 * h + s) * 64 + lg * 16);
#pragma unroll
            for (int s = 0; s < 4; ++s) { q0 = mfma(wf[8 * h + 2 * s], vb[s], q0); q1 = mfma(wf[8 * h + 2 * s + 1], vb[s], q1); }
        }
#pragma unroll
        for (int r = 0; r < 4; ++r) {
            cs[0][r] = dpp0<0xA0>(q0[r]); cs[1][r] = dpp0<0xA0>(q1[r]);
            bs[0][r] = dpp0<0xF5>(q0[r]) + ((r & 1) ? hi2f(rb1[r >> 1]) : lo2f(rb1[r >> 1]));
            bs[1][r] = dpp0<0xF5>(q1[r]) + ((r & 1) ? hi2f(rb1[2 + (r >> 1)]) : lo2f(rb1[2 + (r >> 1)]));
        }
        asm volatile("" : "+v"(cs[0]), "+v"(cs[1]), "+v"(bs[0]), "+v"(bs[1]));
    }
    unsigned sl[2][4], sh[2][4];
    {
        const u4 sv0 = *(LAS u4*)(sm + LD_VEC + ((16 * pg - 128 * cl) & 511) * 2), sv1 = *(LAS u4*)(sm + LD_VEC + ((16 * pg - 128 * cl) & 511) * 2 + 16);
#pragma unroll
        for (int i = 0; i < 4; ++i) { sl[0][i] = sv0[i] & 0xffffu; sh[0][i] = sv0[i] & 0xffff0000u; sl[1][i] = sv1[i] & 0xffffu; sh[1][i] = sv1[i] & 0xffff0000u; }
    }
    pin();
    STAMP(14);

    RETRY(3, badB(), ldB());
    STAMP(15);
    {
        LAS unsigned char* af = sm + LD_AF + (((pg >> 1) - 4 * cl) & 15) * 1024 + (2 * (pg & 1) * 16 + l15) * 16;
#pragma unroll
        for (int h = 0; h < 2; ++h)
#pragma unroll
            for (int r = 0; r < 4; ++r) *(LAS u4*)(af + r * 16384 + h * 256) = scale4(xb[4 * h][r], xb[4 * h + 1][r], xb[4 * h + 2][r], xb[4 * h + 3][r], sl[h], sh[h]);
        sb0();
        float sum[4] = {0, 0, 0, 0}, sq[4] = {0, 0, 0, 0};
#pragma unroll
        for (int i = 0; i < 8; ++i)
#pragma unroll
            for (int r = 0; r < 4; ++r) { sum[r] = dot2(xb[i][r], 0x3f803f80u, sum[r]); sq[r] = dot2(xb[i][r], xb[i][r], sq[r]); }
        const float e0 = swap32_add(sum[0], sum[2]), e1 = swap32_add(sum[1], sum[3]), e2 = swap32_add(sq[0], sq[2]), e3 = swap32_add(sq[1], sq[3]);
        *(LAS f2*)(sm + LD_STATP + w * 512 + (l15 * 4 + lg) * 8) = f2{swap16_add(e0, e1), swap16_add(e2, e3)};
    }
    bar();
    STAMP(16);
    auto ldw2 = [&](int n) { int i = (n >> 2) + rot7; i = i >= 7 ? i - 7 : i; wf[n] = ld4c(rw, lane * 16, w2base + i * 4096 + (n & 3) * 1024); };
    f4 ac[2][4];
    if (opq(w) != 7) {
        u4 hb[2][4];
#pragma unroll
        for (int r = 0; r < 4; ++r) hb[0][r] = *(LAS u4*)(sm + LD_AF + r * 16384 + lane * 16);
        sb0();
#pragma unroll
        for (int s = 0; s < 16; ++s) {
#pragma unroll
            for (int r = 0; r < 4; ++r) {
                if (s < 15) hb[(s + 1) & 1][r] = *(LAS u4*)(sm + LD_AF + r * 16384 + (s + 1) * 1024 + lane * 16);
                if (s >= 1 && s < 15 && (r & 1)) ldw2(2 * (s - 1) + (r >> 1));
                ac[0][r] = mfma(wf[2 * s], hb[s & 1][r], s == 0 ? f4{0, 0, 0, 0} : ac[0][r]);
                ac[1][r] = mfma(wf[2 * s + 1], hb[s & 1][r], s == 0 ? f4{0, 0, 0, 0} : ac[1][r]);
                sb0();
            }
        }
    }
    if (opq(w) == 7) {
        auto ldR = [&]() {
#pragma unroll
            for (int i = 0; i < 8; ++i) xb[i] = ld4c(rw, lg * 2048 + l15 * 16 + i * 256, WS_XB + pan2 * XB_STR + j2 * 8192);
        };
        ldR();
        f2 t = {0.f, 0.f};
#pragma unroll
        for (int i = 0; i < 8; ++i) t += *(LAS f2*)(sm + LD_STATP + i * 512 + lane * 8);
        const float m = t.x * (1.f / D), var = rbf(fmaxf(t.y * (1.f / D) - m * m, 0.f));
        const float rs = __builtin_amdgcn_rcpf(rbf(__builtin_amdgcn_sqrtf(rbf(var + 1e-5f))));
        *(LAS f2*)(sm + LD_STATS + lane * 8) = f2{rs, -rbf(m)};
        RETRY(6, badB(), ldR());
#pragma unroll
        for (int r = 0; r < 4; ++r) {
            LAS unsigned char* xr = sm + LD_XRES + (4 * l15 + r) * 128 + lg * 32;
            *(LAS u4*)xr = u4{xb[0][r], xb[1][r], xb[2][r], xb[3][r]}; *(LAS u4*)(xr + 16) = u4{xb[4][r], xb[5][r], xb[6][r], xb[7][r]};
        }
    }
    STAMP(17);
    bar();
    if (opq(w) != 7) {
        const f4 st0 = *(LAS f4*)(sm + LD_STATS + l15 * 32), st1 = *(LAS f4*)(sm + LD_STATS + l15 * 32 + 16);
        const float rs[4] = {st0[0], st0[2], st1[0], st1[2]}, nm[4] = {st0[1], st0[3], st1[1], st1[3]};
#pragma unroll
        for (int r = 0; r < 4; ++r) {
            u4 o;
#pragma unroll
            for (int q = 0; q < 4; ++q) {
                const int e = q >> 1, c = (q & 1) * 2;
                const float v0 = (ac[e][r][c] + nm[r] * cs[e][c]) * rs[r] + bs[e][c], v1 = (ac[e][r][c + 1] + nm[r] * cs[e][c + 1]) * rs[r] + bs[e][c + 1];
                const float r0 = fmaxf(v0, 0.f), r1 = fmaxf(v1, 0.f);
                o[q] = pk(r0 * r0, r1 * r1);
            }
            st4c(o, rw, lane * 16 + r * 1024, WS_XC + pan1 * XC_STR + (7 * j1 + w) * 4096);
            sb0();
        }
        STAMP(23);
        drain();
        unsigned arr = 0;
        if (lane == 0) arr = __hip_atomic_fetch_add((LAS unsigned*)(sm + LD_CNT), 1u, __ATOMIC_RELAXED, __HIP_MEMORY_SCOPE_WORKGROUP);
        if (__builtin_amdgcn_readfirstlane(arr) == 6u) st2c(u2{tag, ~tag}, rw, lane < 8 ? lane * 128 : 0x40000000, WS_FLC + (pan1 * 8 + j1) * 1024);
    }
    const u4 rb2 = gld<u4>(P.b2, (64 * j2 + 8 * (lane & 7)) * 2);
    pin();
    STAMP(18);

    {
        int ko7[7];
#pragma unroll
        for (int i = 0; i < 7; ++i) { int k = i + rot7; k = k >= 7 ? k - 7 : k; ko7[i] = WS_XC + pan2 * XC_STR + (7 * w + k) * 4096; }
        u4 hm[3][7];
        auto ldg = [&](u4 (&h)[7], int rt) {
#pragma unroll
            for (int i = 0; i < 7; ++i) h[i] = ld4c(rw, lane * 16 + ((rt + j2) & 3) * 1024, ko7[i]);
        };
        u2 fc;
        auto ldC = [&]() { fc = ld2c(rw, 0, WS_FLC + (pan2 * 8 + w) * 1024 + j2 * 128); };
        if (W7) __builtin_amdgcn_s_sleep(24);
        ldC();
        RETRY(4, ((fc.x ^ tag) | (~fc.y ^ tag)) != 0, ldC());
        pin();
        STAMP(19);
        ldg(hm[0], 0); ldg(hm[1], 1); ldg(hm[2], 2);
        pin();
#pragma unroll
        for (int rt = 0; rt < 4; ++rt) {
            f4 ac[4];
#pragma unroll
            for (int i = 0; i < 7; ++i)
#pragma unroll
                for (int m = 0; m < 4; ++m) ac[m] = mfma(wf[4 * i + m], hm[rt % 3][i], i == 0 ? f4{0, 0, 0, 0} : ac[m]);
            sb0();
            if (rt == 0) ldg(hm[0], 3);
            LAS unsigned char* pp = sm + LD_PART + w * 16384 + ((rt + j2) & 3) * 4096 + lg * 256;
#pragma unroll
            for (int m = 0; m < 4; ++m) *(LAS f4*)(pp + m * 1024 + ((l15 + 4 * lg) & 15) * 16) = ac[m];
            pin();
        }
    }
    STAMP(20);
    bar();
    {
        const int rt = w >> 1, tp = 8 * (w & 1) + (lane >> 3), ck = lane & 7, g = ck & 3;
        LAS unsigned char* pp = sm + LD_PART + rt * 4096 + (ck >> 2) * 2048 + (g * 16 + ((tp + 4 * g) & 15)) * 16;
        f4 s0 = {0, 0, 0, 0}, s1 = {0, 0, 0, 0};
#pragma unroll
        for (int i = 0; i < 8; ++i) { s0 += *(LAS f4*)(pp + i * 16384); s1 += *(LAS f4*)(pp + i * 16384 + 1024); }
        const int row = 4 * tp + rt;
        const u4 xr = *(LAS u4*)(sm + LD_XRES + row * 128 + ck * 16);
        const f4 g0 = *(LAS f4*)(sm + LD_VEC + 2048 + ck * 32), g1 = *(LAS f4*)(sm + LD_VEC + 2048 + ck * 32 + 16);
        u4 o;
        o.x = pk(lo2f(xr.x) + g0[0] * (s0[0] + lo2f(rb2.x)), hi2f(xr.x) + g0[1] * (s0[1] + hi2f(rb2.x)));
        o.y = pk(lo2f(xr.y) + g0[2] * (s0[2] + lo2f(rb2.y)), hi2f(xr.y) + g0[3] * (s0[3] + hi2f(rb2.y)));
        o.z = pk(lo2f(xr.z) + g1[0] * (s1[0] + lo2f(rb2.z)), hi2f(xr.z) + g1[1] * (s1[1] + hi2f(rb2.z)));
        o.w = pk(lo2f(xr.w) + g1[2] * (s1[2] + lo2f(rb2.w)), hi2f(xr.w) + g1[3] * (s1[3] + hi2f(rb2.w)));
        *(GAS u4*)((GAS unsigned char*)P.out + (unsigned)((64 * pan2 + row) * D + 64 * j2 + 8 * ck) * 2u) = o;
    }
}

}

extern "C" void kernel_launch(void* const* d_in, const int* in_sizes, int n_in, void* d_out, int out_size, void* d_ws, size_t ws_size, hipStream_t stream) {
    using namespace mk;
    Params P;
    P.x = (const u16*)d_in[0]; P.Are = (const u16*)d_in[1]; P.Aim = (const u16*)d_in[2]; P.Bre = (const u16*)d_in[3]; P.Bim = (const u16*)d_in[4];
    P.Cre = (const u16*)d_in[5]; P.Cim = (const u16*)d_in[6]; P.logdt = (const u16*)d_in[7]; P.dskip = (const u16*)d_in[8];
    P.w1 = (const u16*)d_in[10]; P.b1 = (const u16*)d_in[11]; P.w2 = (const u16*)d_in[12]; P.b2 = (const u16*)d_in[13];
    P.c = (const u16*)d_in[14]; P.wmod = (const u16*)d_in[15];
    P.out = (u16*)d_out; P.ws = (unsigned char*)d_ws;
    (void)hipFuncSetAttribute((const void*)fused, hipFuncAttributeMaxDynamicSharedMemorySize, LD_END);
    hipLaunchKernelGGL(fused, dim3(256), dim3(512), LD_END, stream, P);
}
```

```cpp
#include <hip/hip_runtime.h>
#include <hip/hip_bf16.h>
#include <stdint.h>
#include <type_traits>

#define EXP 0
#define STAMPS 0

namespace mk {

typedef unsigned short u16;
typedef unsigned u4 __attribute__((ext_vector_type(4)));
typedef unsigned u3 __attribute__((ext_vector_type(3)));
typedef unsigned u2 __attribute__((ext_vector_type(2)));
typedef float f4 __attribute__((ext_vector_type(4)));
typedef float f16v __attribute__((ext_vector_type(16)));
typedef float f2 __attribute__((ext_vector_type(2)));
typedef __bf16 bf2 __attribute__((ext_vector_type(2)));
typedef __bf16 bf8 __attribute__((ext_vector_type(8)));
typedef short s4 __attribute__((ext_vector_type(4)));
typedef __amdgpu_buffer_rsrc_t rsrc_t;
#define LAS __attribute__((address_space(3)))
#define GAS __attribute__((address_space(1)))
#define CAS __attribute__((address_space(4)))
#define DEV __device__ __forceinline__

constexpr int L = 2048, D = 512, NM = 128, F = 1792;
constexpr int WS_EP = 0, WS_F2 = 4096, WS_M2 = 8192, WS_ST = 16384, WS_WFLAG = 32768, WS_MODG = 65536, WS_FLC = 196608;
constexpr int XA_STR = 16384 + 4096 + 256 + 128, XB_STR = 65536 + 4096 + 256, XC_STR = 229376;
constexpr int WS_XA = WS_FLC + 32 * 64 * 128, WS_XB = WS_XA + 256 * XA_STR, WS_XC = WS_XB + 32 * XB_STR, WS_W1F = WS_XC + 32 * XC_STR;
constexpr int WS_W2F = WS_W1F + D * F * 2, WS_END = WS_W2F + D * F * 2;
constexpr int WS_STAMP = 32 << 20;
constexpr int NSTAMP = 24;
constexpr int UV_ROW = 544, UV_CH = 63 * UV_ROW;
constexpr int LD_UV = 0;
constexpr int LD_RED = 0;
constexpr int LD_AF = 0;
constexpr int LD_PART = 0;
constexpr int KC_CP = 4160, KC_CH = 2 * KC_CP;
constexpr int LD_KC = 68608;
constexpr int LD_WT = LD_KC + 16640;
constexpr int LD_XT = LD_WT + 16384;
constexpr int LD_MRED = LD_XT + 9216;
constexpr int LD_STATP = LD_MRED + 2560;
constexpr int LD_W7 = LD_KC;
constexpr int LD_STATS = 131072;
constexpr int LD_ZERO = LD_STATS + 1024;
constexpr int LD_HB = LD_ZERO + 512;
constexpr int LD_XBUF = LD_HB + 9216;
constexpr int LD_XRES = LD_XBUF + 8192;
constexpr int LD_VEC = LD_XRES + 8192;
constexpr int LD_CNT = LD_VEC + 2048 + 256;
constexpr int LD_TOT = LD_CNT + 16;
constexpr int LD_GC = LD_TOT + 64;
constexpr int LD_CN2 = LD_GC + 16;
constexpr int LD_TM = LD_CN2 + 16;
constexpr int LD_END = LD_TM + 32;
static_assert(LD_W7 + 28672 <= LD_STATP && LD_STATP + 4096 <= 131072 && LD_END <= 163840, "LDS");
#define CODE_PF_LINES 190
constexpr int AUX_SC1 = 16;
constexpr unsigned SENT = 0xFFFFFFFFu;
constexpr int OOB = 0x40000000;
constexpr int SPIN_CAP = 40000;
#define KNWA 4
#define KNW0 2
#define KNWC 9
#define KNWB 8
#define KHW 8
#define KLDM 34
#define KLDG 8
#define KCHG 8
#define KLFF 12
#define FILLV 2
#define KTAIL 14
#define W2V 2
#define XCV 0
#define KCHF 2
#define KCS0 7
#define KQ2 6
#define KES 1
#define KGO 260
#define KQE 50
#define KE1 10
#define KW2IN 22

DEV float asf(unsigned v) { return __builtin_bit_cast(float, v); }
DEV unsigned asu(float v) { return __builtin_bit_cast(unsigned, v); }
DEV float lo2f(unsigned v) { return asf(v << 16); }
DEV float hi2f(unsigned v) { return asf(v & 0xffff0000u); }
DEV unsigned pk(float a, float b) { bf2 v = {(__bf16)a, (__bf16)b}; return __builtin_bit_cast(unsigned, v); }
DEV unsigned pk(f2 a) { return pk(a.x, a.y); }
DEV float rbf(float a) { return lo2f(pk(a, a)); }
DEV f4 mfma(u4 a, u4 b, f4 c) { return __builtin_amdgcn_mfma_f32_16x16x32_bf16(__builtin_bit_cast(bf8, a), __builtin_bit_cast(bf8, b), c, 0, 0, 0); }
DEV void mfmaa(f4& c, const u4& a, const u4& b) { asm volatile("v_mfma_f32_16x16x32_bf16 %0, %1, %2, %0" : "+v"(c) : "v"(a), "v"(b)); }
DEV void mfma0(f4& c, const u4& a, const u4& b) { asm volatile("v_mfma_f32_16x16x32_bf16 %0, %1, %2, 0" : "=&v"(c) : "v"(a), "v"(b)); }
DEV void mfmaA(f16v& c, const u4& a, const u4& b) { asm volatile("v_mfma_f32_32x32x16_bf16 %0, %1, %2, %0" : "+v"(c) : "v"(a), "v"(b)); }
DEV void mfmaZ(f16v& c, const u4& a, const u4& b) { asm volatile("v_mfma_f32_32x32x16_bf16 %0, %1, %2, 0" : "=&v"(c) : "v"(a), "v"(b)); }
DEV void mfma_done12() { __builtin_amdgcn_sched_barrier(0); asm volatile("s_nop 11" ::: "memory"); __builtin_amdgcn_sched_barrier(0); }
DEV void mfma_done() { __builtin_amdgcn_sched_barrier(0); asm volatile("s_nop 7" ::: "memory"); __builtin_amdgcn_sched_barrier(0); }
constexpr int tri_k(int n) { int k = 0; while ((k + 1) * (16 - k) / 2 <= n) ++k; return k; }
template <int CTRL, int RM = 0xf> DEV float dpp0(float x) { return asf((unsigned)__builtin_amdgcn_update_dpp(0, (int)asu(x), CTRL, RM, 0xf, true)); }
DEV float sum8(float x) { x += dpp0<0xB1>(x); x += dpp0<0x4E>(x); return x + dpp0<0x141>(x); }
DEV float swap32_add(float a, float b) { auto r = __builtin_amdgcn_permlane32_swap(asu(a), asu(b), false, false); return asf(r[0]) + asf(r[1]); }
DEV float swap16_add(float a, float b) { auto r = __builtin_amdgcn_permlane16_swap(asu(a), asu(b), false, false); return asf(r[0]) + asf(r[1]); }
DEV float dot2(unsigned a, unsigned b, float c) { return __builtin_amdgcn_fdot2_f32_bf16(__builtin_bit_cast(bf2, a), __builtin_bit_cast(bf2, b), c, false); }
DEV void sb0() { __builtin_amdgcn_sched_barrier(0); }
DEV void bar() { sb0(); asm volatile("s_waitcnt lgkmcnt(0)\n\ts_barrier" ::: "memory"); sb0(); }
DEV void drain() { asm volatile("s_waitcnt vmcnt(0)" ::: "memory"); }
template <int N> DEV void waitvm() { asm volatile("s_waitcnt vmcnt(%0)" :: "n"(N) : "memory"); }
DEV void pin() { asm volatile("" ::: "memory"); sb0(); }
template <class T> DEV void keep(T& v) { asm volatile("" : "+v"(v)); }
template <int I, int N, class Fn> DEV void sfor(Fn&& f) { if constexpr (I < N) { f(std::integral_constant<int, I>{}); sfor<I + 1, N>(f); } }
DEV int opq(int v) { asm volatile("" : "+s"(v)); return v; }
DEV bool any_bad(bool bad) { return __builtin_amdgcn_ballot_w64(bad) != 0ull; }
DEV rsrc_t mkrs(const void* p, int bytes) { return __builtin_amdgcn_make_buffer_rsrc((void*)p, 0, bytes, 0x00020000); }
DEV u4 ld4(rsrc_t r, int vo, int so) { return __builtin_amdgcn_raw_buffer_load_b128(r, vo, so, (EXP & 1) ? 2 : 0); }
DEV u4 ld4c(rsrc_t r, int vo, int so) { return __builtin_amdgcn_raw_buffer_load_b128(r, vo, so, AUX_SC1); }
DEV u2 ld2c(rsrc_t r, int vo, int so) { return __builtin_amdgcn_raw_buffer_load_b64(r, vo, so, AUX_SC1); }
DEV void st4c(u4 v, rsrc_t r, int vo, int so) { asm("" : "+v"(v)); __builtin_amdgcn_raw_buffer_store_b128(v, r, vo, so, AUX_SC1); asm volatile("s_nop 1" :: "v"(v) : "memory"); }
DEV void st2c(u2 v, rsrc_t r, int vo, int so) { __builtin_amdgcn_raw_buffer_store_b64(v, r, vo, so, AUX_SC1); }
template <class T> DEV T gld(const void* base, unsigned off) { return *(const GAS T*)((const GAS unsigned char*)base + off); }
template <class T> DEV T gldn(const void* base, unsigned off) { if (EXP & 1) return __builtin_nontemporal_load((const GAS T*)((const GAS unsigned char*)base + off)); return gld<T>(base, off); }
DEV unsigned sld(const void* base, unsigned off) { return *(const CAS unsigned*)((const CAS unsigned char*)base + off); }
DEV unsigned umax3(unsigned a, unsigned b, unsigned c) { return max(max(a, b), c); }
DEV u4 trfrag(LAS unsigned char* a, int step) {
    const u2 t0 = __builtin_bit_cast(u2, __builtin_amdgcn_ds_read_tr16_b64_v4i16((LAS s4*)a)), t1 = __builtin_bit_cast(u2, __builtin_amdgcn_ds_read_tr16_b64_v4i16((LAS s4*)(a + step)));
    return u4{t0.x, t0.y, t1.x, t1.y};
}
DEV unsigned lds_ld(LAS unsigned char* a) { return __hip_atomic_load((LAS unsigned*)a, __ATOMIC_RELAXED, __HIP_MEMORY_SCOPE_WORKGROUP); }
DEV void lds_wait(LAS unsigned char* a, unsigned want) {
    while ((unsigned)__builtin_amdgcn_readfirstlane(lds_ld(a)) < want) __builtin_amdgcn_s_sleep(1);
    pin();
}
DEV unsigned lds_arrive(LAS unsigned char* a, int lane) {
    unsigned r = 0;
    pin();
    if (lane == 0) r = __hip_atomic_fetch_add((LAS unsigned*)a, 1u, __ATOMIC_RELAXED, __HIP_MEMORY_SCOPE_WORKGROUP);
    return __builtin_amdgcn_readfirstlane(r);
}
DEV void lds_add1(LAS unsigned char* a) { asm volatile("ds_add_u32 %0, %1" :: "v"((unsigned)(unsigned long long)a), "v"(1u) : "memory"); }
DEV f2 rot(f2 b) { return f2{-b.y, b.x}; }
DEV f2 cmul(f2 a, f2 b, f2 brot) { return __builtin_elementwise_fma(a.xx, b, a.yy * brot); }
DEV f2 csq(f2 a, f2 arot) { return cmul(a, a, arot); }
DEV f2 zpow(float a2, float wr, float e) {
    const float m = __builtin_amdgcn_exp2f(a2 * e), ph = __builtin_amdgcn_fractf(wr * e);
    return f2{m * __builtin_amdgcn_cosf(ph), m * __builtin_amdgcn_sinf(ph)};
}
DEV int urow(int blk, int r) {
    const int lb = (int)lo2f(pk((float)(256 * blk + r), 0.f)) - 256 * blk;
    const int sh = blk == 0 ? 0 : (blk == 1 ? 1 : (blk < 4 ? 2 : 3)), base = blk == 0 ? 0 : (blk == 1 ? 16 : (blk < 4 ? 15 + 5 * blk : 23 + 3 * blk));
    return base + (lb >> sh);
}
DEV u4 scale4(unsigned x0, unsigned x1, unsigned x2, unsigned x3, const unsigned (&sl)[4], const unsigned (&sh)[4]) {
    unsigned o0, o1, o2, o3, t0, t1, t2, t3;
    asm("v_dot2_f32_bf16 %0, %8, %12, 0\n\tv_dot2_f32_bf16 %4, %8, %16, 0\n\tv_dot2_f32_bf16 %1, %9, %13, 0\n\tv_dot2_f32_bf16 %5, %9, %17, 0\n\t"
        "v_dot2_f32_bf16 %2, %10, %14, 0\n\tv_dot2_f32_bf16 %6, %10, %18, 0\n\tv_dot2_f32_bf16 %3, %11, %15, 0\n\tv_dot2_f32_bf16 %7, %11, %19, 0\n\t"
        "v_cvt_pk_bf16_f32 %0, %0, %4\n\tv_cvt_pk_bf16_f32 %1, %1, %5\n\tv_cvt_pk_bf16_f32 %2, %2, %6\n\tv_cvt_pk_bf16_f32 %3, %3, %7"
        : "=&v"(o0), "=&v"(o1), "=&v"(o2), "=&v"(o3), "=&v"(t0), "=&v"(t1), "=&v"(t2), "=&v"(t3)
        : "v"(x0), "v"(x1), "v"(x2), "v"(x3), "v"(sl[0]), "v"(sl[1]), "v"(sl[2]), "v"(sl[3]), "v"(sh[0]), "v"(sh[1]), "v"(sh[2]), "v"(sh[3]));
    return u4{o0, o1, o2, o3};
}

struct Params {
    const u16 *x, *Are, *Aim, *Bre, *Bim, *Cre, *Cim, *logdt, *dskip, *w1, *b1, *w2, *b2, *c, *wmod;
    u16* out;
    unsigned char* ws;
};

#define STAMP(i) do { } while (0)
#define COUNT(id)
#define RETRY(id, BAD, RELOAD) do { if (__builtin_expect(any_bad(BAD), 0)) { int spin_ = 0; do { asm volatile("; retry " #id ::: "memory"); __builtin_amdgcn_s_sleep(2); RELOAD; COUNT(id); } while (any_bad(BAD) && ++spin_ < SPIN_CAP); } } while (0)

__global__ void __launch_bounds__(512, 2) fused(Params P) {
    extern __shared__ __attribute__((aligned(16))) unsigned char smem_[];
    LAS unsigned char* const sm = (LAS unsigned char*)smem_;
    STAMP(0);
    __builtin_amdgcn_s_setprio(3);
    const int tid = threadIdx.x, lane = tid & 63, w = __builtin_amdgcn_readfirstlane(tid >> 6);
    const int l15 = lane & 15, lg = lane >> 4;
    const int b = blockIdx.x, g8 = b & 7, idx = b >> 3, p = g8 * 32 + idx, cl = idx >> 3;
    const int pan1 = 8 * (g8 >> 1) + (idx >> 2), j1 = 4 * (g8 & 1) + (idx & 3);
    const int pan2 = p >> 3, j2 = p & 7;
    unsigned pfv = 0;
    if (w == 6) {
        unsigned long long pc; asm volatile("s_getpc_b64 %0" : "=s"(pc));
        const unsigned o = (unsigned)((lane & 7) * 32 + idx);
        pfv = gld<unsigned>((const void*)(pc & ~127ull), o < CODE_PF_LINES ? o * 128u : 0u);
    }
    asm volatile("" :: "s"(P.x), "s"(P.Are), "s"(P.Aim), "s"(P.Bre), "s"(P.Bim), "s"(P.Cre), "s"(P.Cim), "s"(P.logdt), "s"(P.w1), "s"(P.c), "s"(P.wmod), "s"(P.ws));

    const rsrc_t rw = mkrs(P.ws, WS_END);
    const int kch = (w >> 1) & 1, kd = 2 * p + kch;
    const unsigned ep = sld(P.ws, WS_EP), rdt2 = sld(P.logdt, 4 * p), rds = sld(P.dskip, 4 * p);
    const int wB = 4 * b + (w & 3), wS = wB / 56, wF = wB - 56 * wS;
    u4 wb[2], wc[2];
    auto ldW = [&](u4 (&v)[2], const u16* src, int pitch, int base) {
        const rsrc_t rsw = mkrs(src, D * F * 2);
        const int vo = (lane >> 2) * pitch + (lane & 3) * 16;
        v[0] = ld4(rsw, vo, base); v[1] = ld4(rsw, vo, base + 16 * pitch);
    };
    auto stW = [&](u4 (&v)[2], int dst, bool big) {
        LAS unsigned char* wt = sm + (big ? LD_WT : LD_XT) + w * 2048;
        *(LAS u4*)(wt + lane * 16) = v[0]; *(LAS u4*)(wt + 1024 + lane * 16) = v[1];
        LAS unsigned char* a = wt + (big ? (8 * (lg >> 1) + (l15 >> 2)) * 64 + (lg & 1) * 32 + (lane & 1) * 16 + (lane & 2) * 4 : (8 * lg + (l15 >> 2)) * 64 + (lane & 3) * 16);
        const u4 f0 = trfrag(a, 4 * 64), f1 = trfrag(a + (big ? 1024 : 8), 4 * 64);
        st4c(f0, rw, lane * 16, b < 224 ? dst : OOB); st4c(f1, rw, lane * 16 + 1024, b < 224 ? dst : OOB);
    };
    auto ldW2 = [&]() { ldW(wc, P.w2, D * 2, (32 * (wB >> 4) * D + 32 * (wB & 15)) * 2); };
    const unsigned ko = (unsigned)(kd * NM + (w & 1) * 64 + lane) * 2u;
    const unsigned rAre = gld<u16>(P.Are, ko), rAim = gld<u16>(P.Aim, ko);
    const unsigned rBre = gld<u16>(P.Bre, ko), rBim = gld<u16>(P.Bim, ko), rCre = gld<u16>(P.Cre, ko), rCim = gld<u16>(P.Cim, ko);
    const u4 xblk = gldn<u4>(P.x, (unsigned)((8 * p + (lane >> 3)) * D + 64 * w + 8 * (lane & 7)) * 2u);
    const bool ismod = b >= 64;
    const int mcb = (b - 64) >> 2, mq = b & 3, mr = 128 * mq + 32 * (w & 3) + 4 * (lane >> 3);
    const u2 rc = gld<u2>(P.c, mr * 2);
    pin();
    __builtin_amdgcn_s_setprio(0);
    u4 wm[4];
    {
        const bool lo = w < 4;
        const rsrc_t r0 = mkrs(lo ? P.w1 : P.wmod, lo ? D * F * 2 : (ismod ? 6 * D * D * 2 : 0)), r1 = mkrs(lo ? P.w2 : P.wmod, lo ? ((EXP & 4) ? 0 : D * F * 2) : (ismod ? 6 * D * D * 2 : 0));
        const int vm = (mr * (6 * D) + 8 * (lane & 7)) * 2, sm_ = ismod ? 128 * mcb : 0;
        const int v0 = lo ? (lane >> 2) * (F * 2) + (lane & 3) * 16 : vm, v1 = lo ? (lane >> 2) * (D * 2) + (lane & 3) * 16 : vm;
        const int b0 = (32 * wS * F + 32 * wF) * 2, b1 = (32 * (wB >> 4) * D + 32 * (wB & 15)) * 2;
        wm[0] = ld4(r0, v0, lo ? b0 : sm_); wm[1] = ld4(r0, v0, lo ? b0 + 16 * F * 2 : sm_ + 6 * D * 2);
        wm[2] = ld4(r1, v1, lo ? b1 : sm_ + 2 * 6 * D * 2); wm[3] = ld4(r1, v1, lo ? b1 + 16 * D * 2 : sm_ + 3 * 6 * D * 2);
    }
    pin();
    int ur0, ur1;
    {
        ur0 = LD_UV + (w >> 2) * UV_CH + urow(w & 3, 15 - l15) * UV_ROW + lg * 16; ur1 = LD_UV + (w >> 2) * UV_CH + urow(4 + (w & 3), 15 - l15) * UV_ROW + lg * 16;
        asm volatile("" : "+v"(ur0), "+v"(ur1));
    }
    {
        const int a = tid < 96 ? LD_ZERO + (tid >> 6) * 4096 + tid * 16 : (tid < 112 ? LD_KC + ((tid >> 2) & 3) * KC_CP + 4080 + (tid & 3) * 16 : (tid == 112 ? LD_CNT : LD_CN2));
        if (tid <= 113) *(LAS u4*)(sm + a) = u4{0, 0, 0, 0};
    }
    const unsigned tag = (unsigned)opq((int)(ep * 0x9E3779B1u + 0x7F4A7C15u));
    const float dt = rbf(expf(kch ? hi2f(rdt2) : lo2f(rdt2)));
    pin();

    STAMP(1);
    {
        const float a2 = (dt * lo2f(rAre)) * 1.44269504088896341f, wr = (dt * lo2f(rAim)) * 0.15915494309189535f;
        const f2 bc = {dt * lo2f(rBre), dt * lo2f(rBim)};
        const f2 cf = cmul(f2{lo2f(rCre), lo2f(rCim)}, bc, rot(bc));
        const f2 z2 = zpow(a2, wr, 2.f), z2r = rot(z2), z4 = csq(z2, z2r), z4r = rot(z4), z8 = csq(z4, z4r), z8r = rot(z8);
        const f2 zz = zpow(a2, wr, w < 4 ? 1.f : 256.f), zzr = rot(zz);
        LAS unsigned char* t = sm + LD_UV + kch * UV_CH + ((w & 1) * 64 + lane) * 4;
        auto put = [&](int row, f2 v) { *(LAS unsigned*)(t + row * UV_ROW) = pk(v); };
        auto putc = [&](int row, f2 v) { *(LAS unsigned*)(t + row * UV_ROW) = pk(v.x, -v.y); };
        if (w < 4) {
            f2 u[8], v[8];
            v[1] = csq(z8, z8r); v[2] = csq(v[1], rot(v[1]));
            const f2 y2r = rot(v[2]); v[4] = csq(v[2], y2r);
            const f2 y4r = rot(v[4]), y8 = csq(v[4], y4r), y8r = rot(y8);
            u[0] = cf; u[1] = cmul(cf, zz, zzr); u[2] = cmul(u[0], z2, z2r); u[3] = cmul(u[1], z2, z2r);
#pragma unroll
            for (int i = 0; i < 4; ++i) u[4 + i] = cmul(u[i], z4, z4r);
#pragma unroll
            for (int i = 0; i < 8; ++i) { put(i, u[i]); put(8 + i, cmul(u[i], z8, z8r)); }
            v[3] = cmul(v[1], v[2], y2r);
#pragma unroll
            for (int i = 1; i < 4; ++i) v[4 + i] = cmul(v[i], v[4], y4r);
            *(LAS unsigned*)(t + 47 * UV_ROW) = 0x00003f80u; putc(55, y8);
#pragma unroll
            for (int i = 1; i < 8; ++i) { putc(47 + i, v[i]); putc(55 + i, cmul(v[i], y8, y8r)); }
        } else {
            const f2 s2_ = csq(zz, zzr), s2r = rot(s2_), s4_ = csq(s2_, s2r), s4r = rot(s4_);
            f2 e[9], c2[5], c3[5];
            e[0] = cmul(cf, zz, zzr); e[1] = cmul(e[0], z2, z2r); e[2] = cmul(e[0], z4, z4r); e[3] = cmul(e[1], z4, z4r);
#pragma unroll
            for (int i = 0; i < 5; ++i) e[4 + i] = cmul(e[i], z8, z8r);
#pragma unroll
            for (int i = 0; i < 9; ++i) put(16 + i, e[i]);
#pragma unroll
            for (int i = 0; i < 5; ++i) { c2[i] = cmul(e[2 * i], zz, zzr); c3[i] = cmul(e[2 * i], s2_, s2r); put(25 + i, c2[i]); put(30 + i, c3[i]); }
#pragma unroll
            for (int i = 0; i < 3; ++i) { put(35 + i, cmul(c2[2 * i], s2_, s2r)); put(38 + i, cmul(e[4 * i], s4_, s4r)); put(41 + i, cmul(c2[2 * i], s4_, s4r)); put(44 + i, cmul(c3[2 * i], s4_, s4r)); }
        }
    }
    pin();
    STAMP(2);
    wb[0] = wm[0]; wb[1] = wm[1]; wc[0] = wm[2]; wc[1] = wm[3];
    if (opq(w) < 4) stW(wb, WS_W1F + wF * 32768 + wS * 2048, true);
    pin();
    {
        *(LAS u4*)(sm + LD_XT + w * 1152 + (lane >> 3) * 144 + (lane & 7) * 16) = xblk;
        LAS unsigned char* xt = sm + LD_XT + w * 1152 + (lane & 3) * 288 + (lane >> 2) * 4;
        u4 g[2];
#pragma unroll
        for (int i = 0; i < 2; ++i) g[i] = u4{*(LAS unsigned*)(xt + 64 * i), tag, *(LAS unsigned*)(xt + 144 + 64 * i), tag};
        float s = 0.f, q = 0.f;
#pragma unroll
        for (int i = 0; i < 4; ++i) { s = dot2(xblk[i], 0x3f803f80u, s); q = dot2(xblk[i], xblk[i], q); }
#pragma unroll
        for (int i = 0; i < 2; ++i) st4c(g[i], rw, lane * 16 + i * 1024, WS_XA + p * XA_STR + w * 2048);
        *(LAS f2*)(sm + LD_MRED + 2048 + (lane >> 3) * 64 + w * 8) = f2{sum8(s), sum8(q)};
    }
    pin();
    if (opq(w) >= 4) {
        float sc[4] = {lo2f(rc.x), hi2f(rc.x), lo2f(rc.y), hi2f(rc.y)};
#pragma unroll
        for (int i = 0; i < 4; ++i) sc[i] *= __builtin_amdgcn_rcpf(1.f + __expf(-sc[i]));
        const unsigned sp0 = pk(sc[0], sc[1]), sp1 = pk(sc[2], sc[3]);
        float a[8];
#pragma unroll
        for (int i = 0; i < 4; ++i) {
            a[2 * i] = dot2(__builtin_amdgcn_perm(wm[3][i], wm[2][i], 0x05040100u), sp1, dot2(__builtin_amdgcn_perm(wm[1][i], wm[0][i], 0x05040100u), sp0, 0.f));
            a[2 * i + 1] = dot2(__builtin_amdgcn_perm(wm[3][i], wm[2][i], 0x07060302u), sp1, dot2(__builtin_amdgcn_perm(wm[1][i], wm[0][i], 0x07060302u), sp0, 0.f));
        }
        const float e0 = swap32_add(a[0], a[4]), e1 = swap32_add(a[1], a[5]), e2 = swap32_add(a[2], a[6]), e3 = swap32_add(a[3], a[7]);
        float g0 = swap16_add(e0, e2), g1 = swap16_add(e1, e3);
        g0 += dpp0<0x128>(g0); g1 += dpp0<0x128>(g1);
        *(LAS f2*)(sm + LD_MRED + (w & 3) * 256 + (8 * (l15 & 7) + 2 * lg) * 4) = f2{g0, g1};
    }
    STAMP(3);
    bar();
    if (w == 2) {
        f2 s = {0.f, 0.f};
#pragma unroll
        for (int i = 0; i < 4; ++i) s += *(LAS f2*)(sm + LD_MRED + i * 256 + (lane & 31) * 8);
        st4c(u4{asu(s.x), tag, asu(s.y), tag}, rw, (ismod && lane < 32) ? lane * 64 : OOB, WS_MODG + mcb * 2048 + mq * 16);
    }
    if (w == 1) {
        const f2 sv = *(LAS f2*)(sm + LD_MRED + 2048 + lane * 8);
        const float m = sum8(sv.x) * (1.f / D), var = rbf(fmaxf(sum8(sv.y) * (1.f / D) - m * m, 0.f));
        st2c(u2{pk(m, __builtin_amdgcn_sqrtf(rbf(var + 1e-5f))), tag}, rw, (lane & 7) == 0 ? lane : OOB, WS_ST + p * 64);
    }
    pin();
    {
        LAS unsigned char* va = sm + LD_UV + (w >> 2) * UV_CH + (47 + l15) * UV_ROW + lg * 16;
        u4 fu[2][8], fv[8];
#pragma unroll
        for (int s = 0; s < 8; ++s) { fv[s] = *(LAS u4*)(va + s * 64); fu[0][s] = *(LAS u4*)(sm + ur0 + s * 64); fu[1][s] = *(LAS u4*)(sm + ur1 + s * 64); }
        pin();
        pin();
        if (opq(w) < 4) {
            waitvm<2>();
            st2c(u2{~tag, tag}, rw, (lane == 0 && b < 224) ? 0 : OOB, WS_WFLAG + wF * 128 + wS * 8);
        }
        pin();
        f4 k[2] = {{0, 0, 0, (lane == 48 && (w & 3) == 0) ? (w ? hi2f(rds) : lo2f(rds)) : 0.f}, {0, 0, 0, 0}};
#pragma unroll
        for (int s = 0; s < 8; ++s) { k[0] = mfma(fu[0][s], fv[s], k[0]); k[1] = mfma(fu[1][s], fv[s], k[1]); }
        const auto s0 = __builtin_amdgcn_permlane16_swap(pk(k[0][0], k[0][1]), pk(k[1][0], k[1][1]), false, false);
        const auto s1 = __builtin_amdgcn_permlane16_swap(pk(k[0][2], k[0][3]), pk(k[1][2], k[1][3]), false, false);
        const unsigned e01 = s0[0], e23 = s1[0], e45 = s0[1], e67 = s1[1];
        LAS unsigned char* q = sm + LD_KC + (w >> 2) * KC_CH + (2032 - 1024 * (lg & 1) - 256 * (w & 3) - 16 * l15 + 8 * (lg >> 1)) * 2;
        *(LAS u4*)q = u4{e01, e23, e45, e67};
        *(LAS u16*)(q + KC_CP - 2) = (u16)e01;
        *(LAS u3*)(q + KC_CP) = u3{__builtin_amdgcn_alignbit(e23, e01, 16), __builtin_amdgcn_alignbit(e45, e23, 16), __builtin_amdgcn_alignbit(e67, e45, 16)};
        *(LAS u16*)(q + KC_CP + 12) = (u16)(e67 >> 16);
    }
    pin();
    STAMP(4);
    asm volatile("" :: "v"(pfv));
    pin();
    u4 xa[2], st[2]; u2 fl;
    const int xo = (32 * w + (lane >> 2)) * XA_STR + (lane & 3) * 16, so_ = tid * 16 + w * 1024;
    auto ldX = [&]() { xa[0] = ld4c(rw, xo, WS_XA + p * 64); xa[1] = ld4c(rw, xo, WS_XA + p * 64 + 16 * XA_STR); };
    auto ldS = [&]() { st[0] = ld4c(rw, so_, WS_ST); st[1] = ld4c(rw, so_ + 1024, WS_ST); };
    auto ldF = [&]() { fl = ld2c(rw, w < 7 ? w * 128 + l15 * 8 : min(l15 >> 1, 6) * 128 + ((14 + (l15 & 1) + 4 * cl) & 15) * 8, WS_WFLAG + 7 * j1 * 128); };
    ldX();
    STAMP(5);
    bar();
    if (w == 7) {
#pragma unroll
        for (int i = 0; i < 8; ++i) st4c(u4{SENT, SENT, SENT, SENT}, rw, lane * 16 + i * 1024, WS_XB + (p >> 3) * XB_STR + (p & 7) * 8192);
    }
    ldS();
    u4 A[16];
    LAS unsigned char* const ab = sm + LD_KC + (w >> 2) * KC_CH + ((15 - l15) & 1) * KC_CP + ((15 - l15) >> 1) * 4 + (24 + lg - 8 * (w & 3)) * 16;
    auto ldT = [&](int n) { LAS unsigned* ap = (LAS unsigned*)(ab + (228 - 4 * ((n >> 3) + 8 * (n & 7))) * 16); A[n] = u4{ap[0], ap[1], ap[2], ap[3]}; };
#pragma unroll
    for (int n = 0; n < 8; ++n) ldT(n);
    f4 S[2];
    pin();
    STAMP(6);
    RETRY(0, ((xa[0].y ^ tag) | (xa[0].w ^ tag) | (xa[1].y ^ tag) | (xa[1].w ^ tag)) != 0, ldX());
    STAMP(7);
    RETRY(1, ((st[0].y ^ tag) | (st[0].w ^ tag) | (st[1].y ^ tag) | (st[1].w ^ tag)) != 0, { __builtin_amdgcn_s_sleep(4); ldS(); });
    if (w == 7) { drain(); st2c(u2{(unsigned)__builtin_amdgcn_s_memrealtime(), tag}, rw, lane == 0 ? 0 : OOB, WS_F2 + p * 8); }
    u4 mg;
    auto ldG = [&]() { mg = ld4c(rw, (lane >= 32 && b < 96) ? (760 + 8 * b) * 64 + lane * 16 : (min(lane >> 2, 2) * 256 + p) * 64 + (lane & 3) * 16, WS_MODG); };
    if (opq(w) == 3) ldG();
    ldF();
    STAMP(8);
#pragma unroll
    for (int i = 0; i < 2; ++i) {
        const int t = 256 * w + 128 * i + 2 * lane;
        const float m0 = lo2f(st[i].x), r0 = __builtin_amdgcn_rcpf(hi2f(st[i].x)), m1 = lo2f(st[i].z), r1 = __builtin_amdgcn_rcpf(hi2f(st[i].z));
        *(LAS unsigned*)(sm + LD_HB + (256 + t) * 2) = pk((lo2f(xa[i].x) - m0) * r0, (lo2f(xa[i].z) - m1) * r1);
        *(LAS unsigned*)(sm + LD_HB + 4608 + (256 + t) * 2) = pk((hi2f(xa[i].x) - m0) * r0, (hi2f(xa[i].z) - m1) * r1);
        *(LAS u2*)(sm + LD_XBUF + t * 4) = u2{xa[i].x, xa[i].z};
    }
    pin();
    u4 wf[32];
    const int w1base = WS_W1F + 7 * j1 * 32768;
    auto ldw1 = [&](int n) { wf[n] = ld4c(rw, lane * 16, w1base + (w < 7 ? w * 32768 + ((n * 1024 + 8192 * cl) & 32767) : (n >> 2) * 32768 + (((28 + (n & 3)) * 1024 + 8192 * cl) & 32767))); };
    constexpr int NW0 = KNW0, NW2 = NW0 + 2 * KNWC, NW3 = 28;
    static_assert(NW2 <= 28, "weight stream split");
    pin();
    bar();
    STAMP(9);

    u4 mv; u2 f2v;
    f4 x0, x1;
    auto ldM = [&]() { mv = ld4c(rw, w < 4 ? tid * 16 : 4096 + j2 * 256 + l15 * 16, WS_M2); };
    auto ldFF = [&]() { f2v = ld2c(rw, (lane & 31) * 8, WS_F2 + w * 256); };
    {
        f4 acc[8];
        LAS unsigned char* bb = sm + LD_HB + (w >> 2) * 4608 + (208 - 64 * (w & 3) + 16 * l15 + 8 * lg) * 2;
        LAS unsigned char* rdw = sm + LD_RED + w * 8192 + (4 * l15 + (lg ^ ((l15 >> 1) & 3))) * 16;
        u4 B[16];
#pragma unroll
        for (int k = 0; k < 8; ++k) B[k] = *(LAS u4*)(bb + (256 * k + 32) * 2);
        u2 kv[2]; float ts[2];
        kv[0] = *(LAS u2*)(sm + LD_KC + (2044 - 4 * tid) * 2); kv[1] = *(LAS u2*)(sm + LD_KC + KC_CH + (2044 - 4 * tid) * 2);
        const u4 xv = *(LAS u4*)(sm + LD_XBUF + tid * 16);
        pin();
        RETRY(7, ((fl.y ^ tag) | (~fl.x ^ tag)) != 0, ldF());
#pragma unroll
        for (int n = 0; n < NW0; ++n) ldw1(n);
        pin();
        sfor<0, 72>([&](auto mc) __attribute__((always_inline)) {
            constexpr int m = decltype(mc)::value, rr = m / 36, n = m % 36, k = tri_k(n), i = n - (k * (17 - k)) / 2;
            if constexpr (m < 8) mfma0(acc[i + k], A[i], B[k]); else mfmaa(acc[i + k], A[8 * rr + i], B[8 * rr + k]);
            if constexpr (rr == 0 && n % 4 == 0 && n / 4 < 8) ldT(8 + n / 4);
            if constexpr (rr == 0 && n % 4 == 1 && n / 4 < 8) B[8 + n / 4] = *(LAS u4*)(bb + (256 * (n / 4)) * 2);
            if constexpr (m % 4 == 3 && (m / 4) * (NW2 - NW0) / 18 < (m / 4 + 1) * (NW2 - NW0) / 18) ldw1(NW0 + (m / 4) * (NW2 - NW0) / 18);
            if constexpr (m == 38 || m == 40) {
                constexpr int c = (m - 38) / 2;
                S[c][0] = hi2f(kv[c].y); S[c][1] = S[c][0] + lo2f(kv[c].y); S[c][2] = S[c][1] + hi2f(kv[c].x); S[c][3] = S[c][2] + lo2f(kv[c].x);
                keep(S[c]); ts[c] = S[c][3];
            }
            if constexpr (m == 42) { ts[0] += dpp0<0x111>(ts[0]); ts[1] += dpp0<0x111>(ts[1]); keep(ts[0]); keep(ts[1]); }
            if constexpr (m == 43) { ts[0] += dpp0<0x112>(ts[0]); ts[1] += dpp0<0x112>(ts[1]); keep(ts[0]); keep(ts[1]); }
            if constexpr (m == 44) { ts[0] += dpp0<0x114>(ts[0]); ts[1] += dpp0<0x114>(ts[1]); keep(ts[0]); keep(ts[1]); }
            if constexpr (m == 45) { ts[0] += dpp0<0x118>(ts[0]); ts[1] += dpp0<0x118>(ts[1]); keep(ts[0]); keep(ts[1]); }
            if constexpr (m == 46) { ts[0] += dpp0<0x142, 0xa>(ts[0]); ts[1] += dpp0<0x142, 0xa>(ts[1]); keep(ts[0]); keep(ts[1]); }
            if constexpr (m == 48) { ts[0] += dpp0<0x143, 0xc>(ts[0]); ts[1] += dpp0<0x143, 0xc>(ts[1]); keep(ts[0]); keep(ts[1]); }
            if constexpr (m == 50) *(LAS f2*)(sm + (lane == 63 ? LD_TOT + w * 8 : LD_STATP + lane * 8)) = f2{ts[0], ts[1]};
            if constexpr (m == 52 || m == 54) { constexpr int c = (m - 52) / 2; float d = ts[c] - S[c][3]; keep(d); S[c] += d; keep(S[c]); }
            if constexpr (m == 56) { x0 = f4{lo2f(xv.x), lo2f(xv.y), lo2f(xv.z), lo2f(xv.w)}; keep(x0); }
            if constexpr (m == 58) { x1 = f4{hi2f(xv.x), hi2f(xv.y), hi2f(xv.z), hi2f(xv.w)}; keep(x1); }
            if constexpr (m == KCHG) {
                sb0();
                if (opq(w) == 3) {
                    RETRY(5, ((mg.y ^ tag) | (mg.w ^ tag)) != 0, ldG());
                    float m0_ = asf(mg.x), m1_ = asf(mg.z);
                    m0_ += dpp0<0xB1>(m0_); m1_ += dpp0<0xB1>(m1_); m0_ += dpp0<0x4E>(m0_); m1_ += dpp0<0x4E>(m1_);
                    const unsigned mp = pk(m0_, m1_), m1p = pk(1.f + lo2f(mp), 1.f + hi2f(mp));
                    st2c(u2{(b >> 5) == 1 ? m1p : mp, tag}, rw, (lane >= 32 && (lane & 3) == 0 && b < 96) ? lane * 2 : OOB, WS_M2 + b * 64 - 64);
                    const unsigned gap = __builtin_amdgcn_readlane(mp, 8), v = lane ? m1p : mp;
                    float g0 = lo2f(gap) * lo2f(v), g1 = hi2f(gap) * hi2f(v);
                    keep(g0); keep(g1);
                    if ((lane & 59) == 0) *(LAS f2*)(sm + LD_GC + lane * 2) = f2{g0, g1};
                }
            }
            if constexpr (m == 36 + KLFF) ldFF();
            if constexpr (m == 36 + KLDM) ldM();
            if constexpr (rr == 1 && i + k == 7 && k >= 1 && k <= 4) *(LAS f4*)(rdw + (k - 1) * 1024) = acc[k - 1];
            if constexpr (m == 35) STAMP(10);
            sb0();
        });
        *(LAS f4*)(rdw + 4 * 1024) = acc[4];
        mfma_done();
        *(LAS f4*)(rdw + 5 * 1024) = acc[5]; *(LAS f4*)(rdw + 6 * 1024) = acc[6]; *(LAS f4*)(rdw + 7 * 1024) = acc[7];
    }
    RETRY(10, f2v.y != tag, { __builtin_amdgcn_s_sleep(6); ldFF(); });
    {
        const unsigned t0 = __builtin_amdgcn_readfirstlane(f2v.x);
        int d = (int)(f2v.x - t0);
        d = max(d, __builtin_amdgcn_update_dpp(0, d, 0xB1, 0xf, 0xf, true)); d = max(d, __builtin_amdgcn_update_dpp(0, d, 0x4E, 0xf, 0xf, true));
        d = max(d, __builtin_amdgcn_update_dpp(0, d, 0x141, 0xf, 0xf, true)); d = max(d, __builtin_amdgcn_update_dpp(0, d, 0x140, 0xf, 0xf, true));
        *(LAS unsigned*)(sm + LD_TM + w * 4) = t0 + (unsigned)max(__builtin_amdgcn_readlane(d, 0), __builtin_amdgcn_readlane(d, 16));
    }
    STAMP(11);
    bar();
    {
        const f4 gq = *(LAS f4*)(sm + LD_GC);
        const f2 tot = *(LAS f2*)(sm + LD_TOT + (lane & 7) * 8);
        LAS unsigned char* rd = sm + LD_RED + w * 1024 + ((lane & ~3) + ((lane & 3) ^ ((lane >> 3) & 3))) * 16;
        f4 y0 = (*(LAS f4*)(rd) + *(LAS f4*)(rd + 8192)) + (*(LAS f4*)(rd + 2 * 8192) + *(LAS f4*)(rd + 3 * 8192));
        f4 y1 = (*(LAS f4*)(rd + 4 * 8192) + *(LAS f4*)(rd + 5 * 8192)) + (*(LAS f4*)(rd + 6 * 8192) + *(LAS f4*)(rd + 7 * 8192));
        const float o0 = sum8((lane & 7) < w ? tot.x : 0.f), o1 = sum8((lane & 7) < w ? tot.y : 0.f);
        y0 = (x0 + gq[0] * (S[0] + o0)) + gq[2] * y0; y1 = (x1 + gq[1] * (S[1] + o1)) + gq[3] * y1;
        keep(y0); keep(y1);
        u4 o;
#pragma unroll
        for (int r = 0; r < 4; ++r) o[r] = min(pk(y0[r], y1[r]), SENT - 1u);
        st4c(o, rw, lg * XB_STR + l15 * 16, WS_XB + 4 * w * XB_STR + p * 256);
    }
    pin();
    STAMP(12);
    RETRY(2, ((mv.y ^ tag) | (mv.w ^ tag)) != 0, { __builtin_amdgcn_s_sleep(4); ldM(); });
    if (w < 4) *(LAS u2*)(sm + LD_VEC + (w < 2 ? 1024 : 0) + ((4 * tid - 128 * cl) & 511) * 2) = u2{mv.x, mv.z};
    else *(LAS f4*)(sm + LD_VEC + 2048 + l15 * 16) = f4{lo2f(mv.x), hi2f(mv.x), lo2f(mv.z), hi2f(mv.z)};
    unsigned tgo;
    {
        const unsigned tm = *(LAS unsigned*)(sm + LD_TM + (lane & 7) * 4), t0 = __builtin_amdgcn_readfirstlane(tm);
        int d = (int)(tm - t0);
        d = max(d, __builtin_amdgcn_update_dpp(0, d, 0xB1, 0xf, 0xf, true)); d = max(d, __builtin_amdgcn_update_dpp(0, d, 0x4E, 0xf, 0xf, true)); d = max(d, __builtin_amdgcn_update_dpp(0, d, 0x141, 0xf, 0xf, true));
        tgo = t0 + (unsigned)__builtin_amdgcn_readfirstlane(d) + KGO;
    }
    if (b == 0 && tid == 448) __builtin_amdgcn_raw_buffer_store_b32(tag, rw, WS_EP, 0, AUX_SC1);
    bar();
    if (opq(w) < 4) stW(wc, WS_W2F + ((wB & 15) >> 1) * 229376 + (wB >> 4) * 4096 + (wB & 1) * 2048, false);
    pin();
    STAMP(13);
    const unsigned rb1 = gld<u16>(P.b1, (224 * j1 + 32 * min(w, 6) + (lane & 19) + ((lane & 4) << 1) + ((lane & 8) >> 1)) * 2);
    const int pg = tid >> 4;
    u4 xb[8];
    auto ldB = [&]() {
#pragma unroll
        for (int i = 0; i < 8; ++i) xb[i] = ld4c(rw, pg * 2048 + l15 * 16 + i * 256, WS_XB + pan1 * XB_STR);
    };
    auto badB = [&]() {
        unsigned mx = 0;
#pragma unroll
        for (int i = 0; i < 8; ++i) mx = umax3(umax3(mx, xb[i].x, xb[i].y), xb[i].z, xb[i].w);
        return mx == SENT;
    };
    f4 q0;
    const int vo = (l15 >> 1) == 2 * (lg & 1) ? LD_VEC + (lane & 1) * 1024 + (lg >> 1) * 16 : LD_ZERO;
    auto gate = [&]() { sb0(); for (int i = 0; i < 64 && (int)(tgo - (unsigned)__builtin_amdgcn_s_memrealtime()) > 0; ++i) __builtin_amdgcn_s_sleep(1); sb0(); };
    if (opq(w) != 7) {
        u4 vb[8];
#pragma unroll
        for (int n = 0; n < 6; ++n) vb[n] = *(LAS u4*)(sm + vo + n * 32);
        sb0();
        sfor<0, 28>([&](auto nc) __attribute__((always_inline)) {
            constexpr int n = decltype(nc)::value;
            if constexpr (n + 6 < 28) vb[(n + 6) & 7] = *(LAS u4*)(sm + vo + (n + 6) * 32);
            if constexpr (n % 2 == 0 && NW2 + n / 2 < 28) ldw1(NW2 + n / 2);
            if constexpr (n == NW2) { gate(); ldB(); sb0(); }
            if constexpr (n == 0) mfma0(q0, vb[0], wf[0]); else mfmaa(q0, vb[n & 7], wf[n]);
            if constexpr (n == NW2) st2c(u2{~tag, tag}, rw, (lane == 0 && w < 4 && b < 224) ? 0 : OOB, WS_WFLAG + 8192 + ((wB & 15) >> 1) * 896 + (wB >> 4) * 16 + (wB & 1) * 8);
            sb0();
        });
        static_assert(NW2 < 28 && 2 * (28 - NW2) <= NW2, "order");
    }
    if (opq(w) == 7) {
        sfor<0, 28>([&](auto nc) __attribute__((always_inline)) {
            constexpr int n = decltype(nc)::value;
            if constexpr (n % 2 == 0 && NW2 + n / 2 < 28) ldw1(NW2 + n / 2);
            if constexpr (n == NW2) { gate(); ldB(); sb0(); }
            *(LAS u4*)(sm + LD_W7 + n * 1024 + lane * 16) = wf[n];
            sb0();
        });
    }
    pin();
    unsigned sl[2][4], sh[2][4];
    int afo = LD_AF + ((pg - 8 * cl) & 31) * 1024 + l15 * 16;
    const unsigned one = 0x3f803f80u;
    u4 sel = {l15 == 0 ? one : 0u, l15 == 1 ? one : 0u, l15 == 2 ? one : 0u, l15 == 3 ? one : 0u};
    keep(afo); keep(sel);
    {
        const u4 sv0 = *(LAS u4*)(sm + LD_VEC + ((16 * pg - 128 * cl) & 511) * 2), sv1 = *(LAS u4*)(sm + LD_VEC + ((16 * pg - 128 * cl) & 511) * 2 + 16);
#pragma unroll
        for (int i = 0; i < 4; ++i) { sl[0][i] = sv0[i] & 0xffffu; sh[0][i] = sv0[i] & 0xffff0000u; sl[1][i] = sv1[i] & 0xffffu; sh[1][i] = sv1[i] & 0xffff0000u; keep(sl[0][i]); keep(sh[0][i]); keep(sl[1][i]); keep(sh[1][i]); }
    }
    pin();
    STAMP(14);

    RETRY(3, badB(), ldB());
    STAMP(15);
#pragma unroll
    for (int r = 0; r < 4; ++r) {
        const u4 a0 = scale4(xb[0][r], xb[1][r], xb[2][r], xb[3][r], sl[0], sh[0]), a1 = scale4(xb[4][r], xb[5][r], xb[6][r], xb[7][r], sl[1], sh[1]);
        *(LAS u4*)(sm + afo + (r >> 1) * 32768 + (r & 1) * 256) = a0; *(LAS u4*)(sm + afo + (r >> 1) * 32768 + (r & 1) * 256 + 512) = a1;
    }
    pin();
    {
        f4 sm4 = {0, 0, 0, 0};
        float sq[4] = {0, 0, 0, 0};
#pragma unroll
        for (int i = 0; i < 8; ++i) {
            sm4 = mfma(sel, xb[i], sm4);
#pragma unroll
            for (int r = 0; r < 4; ++r) sq[r] = dot2(xb[i][r], xb[i][r], sq[r]);
        }
        *(LAS float*)(sm + LD_STATP + w * 512 + 256 + (l15 * 4 + lg) * 4) = swap16_add(swap32_add(sq[0], sq[2]), swap32_add(sq[1], sq[3]));
        if (lg == 0) *(LAS f4*)(sm + LD_STATP + w * 512 + l15 * 16) = sm4;
    }
    auto ldF2 = [&]() { fl = ld2c(rw, min(lane, 13) * 8, WS_WFLAG + 8192 + j2 * 896 + w * 112); };
    ldF2();
    bar();
    STAMP(16);
    u4 wg[28];
    const int rot7 = 3 * (cl & 1), w2base = WS_W2F + j2 * 229376 + w * 28672;
    auto ldw2 = [&](int n) { int i = (n >> 2) + rot7; i = i >= 7 ? i - 7 : i; wg[n] = ld4c(rw, lane * 16, w2base + i * 4096 + (n & 3) * 1024); };
    auto raise = [&](int ev) {
        if (lds_arrive(sm + LD_CNT + 8 * ev, lane) == 6u) st2c(u2{tag, ~tag}, rw, lane < 8 ? lane * 128 : OOB, WS_FLC + (pan1 * 8 + j1) * 1024 + ev * 64);
    };
    constexpr int W2IN = KW2IN;
    if (opq(w) != 7) {
        f16v ac[2];
        constexpr int T = KTAIL, NH = 64, NK = 2 * (32 - T), PF = 5, Q2 = KQ2, QE = KQE;
        constexpr auto fs = [](int q) constexpr { return q < NK ? q >> 1 : 32 - T + (q - NK) % T; };
        constexpr auto fr = [](int q) constexpr { return q < NK ? q & 1 : (q - NK) / T; };
        u4 hb[8], o[2], aq, bq[2], vl[4];
        float rs[2];
        auto ldhb = [&](int q) { hb[q & 7] = *(LAS u4*)(sm + LD_AF + fr(q) * 32768 + fs(q) * 1024 + lane * 16); };
        auto epi = [&](int r, int c) {
            f4 v = f4{ac[r][4 * c], ac[r][4 * c + 1], ac[r][4 * c + 2], ac[r][4 * c + 3]} * rs[r];
            v = __builtin_elementwise_max(v, f4{0, 0, 0, 0});
            v = v * v;
            keep(v);
            o[c >> 1][2 * (c & 1)] = pk(v[0], v[1]); o[c >> 1][2 * (c & 1) + 1] = pk(v[2], v[3]);
            if (c & 1) st4c(o[c >> 1], rw, (lg & 1) * 1024 + (lg >> 1) * 256 + l15 * 16 + r * 2048 + (c >> 1) * 512, WS_XC + pan1 * XC_STR + (7 * j1 + w) * 4096);
        };
#pragma unroll
        for (int q = 0; q < PF; ++q) { ldhb(q); sb0(); }
        sfor<0, NH>([&](auto qc) __attribute__((always_inline)) {
            constexpr int q = decltype(qc)::value, s = fs(q), r = fr(q);
            if constexpr (q + PF < NH) ldhb(q + PF);
            if constexpr (q >= 2 && q < 6) { wf[26 + q] = *(LAS u4*)(sm + LD_W7 + (4 * w + q - 2) * 1024 + lane * 16); vl[q - 2] = *(LAS u4*)(sm + vo + (26 + q) * 32); }
            constexpr int slot = q - Q2, n0 = slot * W2IN / (QE - Q2), n1 = (slot + 1) * W2IN / (QE - Q2);
            if constexpr (slot == 0) RETRY(8, ((fl.y ^ tag) | (~fl.x ^ tag)) != 0, ldF2());
            if constexpr (slot >= 0 && n1 > n0 && n0 < W2IN) ldw2(n0);
            if constexpr (s == 0) mfmaZ(ac[r], wf[0], hb[q & 7]); else mfmaA(ac[r], wf[s], hb[q & 7]);
            if constexpr (q >= 12 && q < 16) mfmaa(q0, vl[q - 12], wf[16 + q]);
            if constexpr (q == 20) {
                const float bs = lane < 32 ? q0[1] + lo2f(rb1) : 0.f;
                const unsigned hi = pk(q0[0], bs);
                const unsigned lo = pk(q0[0] - lo2f(hi), bs - hi2f(hi));
                aq = u4{__builtin_amdgcn_perm(lo, hi, 0x05040100u), __builtin_amdgcn_perm(lo, hi, 0x07060302u), 0u, 0u};
                keep(aq);
            }
            if constexpr (q == 22) {
                lds_wait(sm + LD_CNT + 4, 1u);
#pragma unroll
                for (int t = 0; t < 2; ++t) bq[t] = *(LAS u4*)(sm + LD_STATS + (l15 * 4 + (lg & 1)) * 16 + t * 32);
            }
            if constexpr (q == 26) { rs[0] = asf(bq[0].z); rs[1] = asf(bq[1].z); keep(rs[0]); keep(rs[1]); bq[0].z = 0u; bq[1].z = 0u; keep(bq[0]); keep(bq[1]); }
            if constexpr (q == 28 || q == 29) mfmaA(ac[r], aq, bq[r]);
            sb0();
            if constexpr (q >= NK + T + 3 && q < NK + T + 3 + 4 * KES && (q - NK - T - 3) % KES == 0) epi(0, (q - NK - T - 3) / KES);
            sb0();
        });
        static_assert(NK > 30 && T >= 11, "schedule");
        STAMP(17);
        drain();
        raise(0);
        mfma_done12();
        epi(1, 0); epi(1, 1); epi(1, 2); epi(1, 3);
        STAMP(23);
    }
    if (opq(w) == 7) {
        auto ldR = [&]() {
#pragma unroll
            for (int i = 0; i < 8; ++i) xb[i] = ld4c(rw, lg * 2048 + l15 * 16 + i * 256, WS_XB + pan2 * XB_STR + j2 * 8192);
        };
        ldR();
        f2 t = {0.f, 0.f};
        LAS unsigned char* sp = sm + LD_STATP + lane * 4;
#pragma unroll
        for (int i = 0; i < 8; ++i) t += f2{*(LAS float*)(sp + i * 512), *(LAS float*)(sp + i * 512 + 256)};
        const float m = t.x * (1.f / D), var = rbf(fmaxf(t.y * (1.f / D) - m * m, 0.f));
        const float sd = __builtin_amdgcn_sqrtf(rbf(var + 1e-5f));
        const unsigned sdp = pk(sd, sd);
        *(LAS u4*)(sm + LD_STATS + lane * 16) = u4{pk(-m, -m), sdp, asu(__builtin_amdgcn_rcpf(lo2f(sdp))), 0u};
        lds_arrive(sm + LD_CNT + 4, lane);
        RETRY(6, badB(), ldR());
#pragma unroll
        for (int r = 0; r < 4; ++r) {
            LAS unsigned char* xr = sm + LD_XRES + (4 * l15 + r) * 128 + lg * 32;
            *(LAS u4*)xr = u4{xb[0][r], xb[1][r], xb[2][r], xb[3][r]}; *(LAS u4*)(xr + 16) = u4{xb[4][r], xb[5][r], xb[6][r], xb[7][r]};
        }
        RETRY(9, ((fl.y ^ tag) | (~fl.x ^ tag)) != 0, ldF2());
        pin();
#pragma unroll
        for (int n = 0; n < W2IN; ++n) { ldw2(n); __builtin_amdgcn_s_sleep(2); sb0(); }
        STAMP(17);
    }
    pin();
#pragma unroll
    for (int n = W2IN; n < 28; ++n) ldw2(n);
    const u4 rb2 = gld<u4>(P.b2, (64 * j2 + 8 * (lane & 7)) * 2);
    lds_wait(sm + LD_CNT, 7u);

    {
        int ko7[7];
#pragma unroll
        for (int i = 0; i < 7; ++i) { int k = i + rot7; k = k >= 7 ? k - 7 : k; ko7[i] = WS_XC + pan2 * XC_STR + (7 * w + k) * 4096; }
        u4 hm[28];
        const int ho = lane * 16 + (j2 & 1) * 1024;
        auto ldh = [&](int n) { hm[n] = ld4c(rw, ho ^ ((n / 7) * 1024), ko7[n % 7]); };
        u2 fc;
        auto ldC = [&](int ev) { fc = ld2c(rw, 0, WS_FLC + (pan2 * 8 + w) * 1024 + j2 * 128 + ev * 64); };
        ldC(0);
        drain();
        if (opq(w) != 7) raise(1);
        STAMP(18);
        RETRY(4, ((fc.x ^ tag) | (~fc.y ^ tag)) != 0, ldC(0));
        pin();
        STAMP(19);
        constexpr int HW = KHW;
        static_assert(HW <= 11, "window");
#pragma unroll
        for (int n = 0; n < HW; ++n) { ldh(n); sb0(); }
        f4 ac[2][4];
        LAS unsigned char* pp = sm + LD_PART + w * 16384 + lg * 256 + ((l15 + 4 * lg) & 15) * 16;
        auto stp = [&](int rt, int m) { *(LAS f4*)(pp + ((rt ^ (j2 & 1)) * 4096) + m * 1024) = ac[rt & 1][m]; };
        sfor<0, 28>([&](auto nc) __attribute__((always_inline)) {
            constexpr int n = decltype(nc)::value, rt = n / 7, i = n % 7;
#pragma unroll
            for (int m = 0; m < 4; ++m) { if (i == 0) mfma0(ac[rt & 1][m], wg[m], hm[n]); else mfmaa(ac[rt & 1][m], wg[4 * i + m], hm[n]); }
            if constexpr (n + HW == KE1) ldC(1);
            if constexpr (n + HW == 14) RETRY(11, ((fc.x ^ tag) | (~fc.y ^ tag)) != 0, ldC(1));
            if constexpr (n + HW < 28) ldh(n + HW);
            if constexpr (rt > 0 && i >= 1 && i <= 4) stp(rt - 1, i - 1);
            sb0();
        });
        mfma_done();
        stp(3, 0); stp(3, 1); stp(3, 2); stp(3, 3);
    }
    STAMP(20);
    bar();
    {
        const int rt = w >> 1, tp = 8 * (w & 1) + (lane >> 3), ck = lane & 7, g = ck & 3;
        LAS unsigned char* pp = sm + LD_PART + rt * 4096 + (ck >> 2) * 2048 + (g * 16 + ((tp + 4 * g) & 15)) * 16;
        f4 s0 = {0, 0, 0, 0}, s1 = {0, 0, 0, 0};
#pragma unroll
        for (int i = 0; i < 8; ++i) { s0 += *(LAS f4*)(pp + i * 16384); s1 += *(LAS f4*)(pp + i * 16384 + 1024); }
        const int row = 4 * tp + rt;
        const u4 xr = *(LAS u4*)(sm + LD_XRES + row * 128 + ck * 16);
        const f4 g0 = *(LAS f4*)(sm + LD_VEC + 2048 + ck * 32), g1 = *(LAS f4*)(sm + LD_VEC + 2048 + ck * 32 + 16);
        u4 o;
        o.x = pk(lo2f(xr.x) + g0[0] * (s0[0] + lo2f(rb2.x)), hi2f(xr.x) + g0[1] * (s0[1] + hi2f(rb2.x)));
        o.y = pk(lo2f(xr.y) + g0[2] * (s0[2] + lo2f(rb2.y)), hi2f(xr.y) + g0[3] * (s0[3] + hi2f(rb2.y)));
        o.z = pk(lo2f(xr.z) + g1[0] * (s1[0] + lo2f(rb2.z)), hi2f(xr.z) + g1[1] * (s1[1] + hi2f(rb2.z)));
        o.w = pk(lo2f(xr.w) + g1[2] * (s1[2] + lo2f(rb2.w)), hi2f(xr.w) + g1[3] * (s1[3] + hi2f(rb2.w)));
        *(GAS u4*)((GAS unsigned char*)P.out + (unsigned)((64 * pan2 + row) * D + 64 * j2 + 8 * ck) * 2u) = o;
    }
}

}

extern "C" void kernel_launch(void* const* d_in, const int* in_sizes, int n_in, void* d_out, int out_size, void* d_ws, size_t ws_size, hipStream_t stream) {
    using namespace mk;
    Params P;
    P.x = (const u16*)d_in[0]; P.Are = (const u16*)d_in[1]; P.Aim = (const u16*)d_in[2]; P.Bre = (const u16*)d_in[3]; P.Bim = (const u16*)d_in[4];
    P.Cre = (const u16*)d_in[5]; P.Cim = (const u16*)d_in[6]; P.logdt = (const u16*)d_in[7]; P.dskip = (const u16*)d_in[8];
    P.w1 = (const u16*)d_in[10]; P.b1 = (const u16*)d_in[11]; P.w2 = (const u16*)d_in[12]; P.b2 = (const u16*)d_in[13];
    P.c = (const u16*)d_in[14]; P.wmod = (const u16*)d_in[15];
    P.out = (u16*)d_out; P.ws = (unsigned char*)d_ws;
    (void)hipFuncSetAttribute((const void*)fused, hipFuncAttributeMaxDynamicSharedMemorySize, LD_END);
    hipLaunchKernelGGL(fused, dim3(256), dim3(512), LD_END, stream, P);
}
```

```cpp
#define XAV 1
#define LXV 1
#define CODE_PF_LINES 156
#include <hip/hip_runtime.h>
#include <hip/hip_bf16.h>
#include <stdint.h>
#include <type_traits>

#define EXP 0
#define STAMPS 0

namespace mk {

typedef unsigned short u16;
typedef unsigned u4 __attribute__((ext_vector_type(4)));
typedef unsigned u3 __attribute__((ext_vector_type(3)));
typedef unsigned u2 __attribute__((ext_vector_type(2)));
typedef float f4 __attribute__((ext_vector_type(4)));
typedef float f16v __attribute__((ext_vector_type(16)));
typedef float f2 __attribute__((ext_vector_type(2)));
typedef __bf16 bf2 __attribute__((ext_vector_type(2)));
typedef __bf16 bf8 __attribute__((ext_vector_type(8)));
typedef short s4 __attribute__((ext_vector_type(4)));
typedef __amdgpu_buffer_rsrc_t rsrc_t;
#define LAS __attribute__((address_space(3)))
#define GAS __attribute__((address_space(1)))
#define CAS __attribute__((address_space(4)))
#define DEV __device__ __forceinline__

constexpr int L = 2048, D = 512, NM = 128, F = 1792;
constexpr int WS_EP = 0, WS_F2 = 4096, WS_M2 = 8192, WS_ST = 16384, WS_WFLAG = 32768, WS_MODG = 65536, WS_FLC = 196608;
constexpr int XA_STR = 16384 + 4096 + 256 + 128, XB_STR = 65536 + 4096 + 256, XC_STR = 229376;
constexpr int WS_XA = WS_FLC + 32 * 64 * 128, WS_XB = WS_XA + 256 * XA_STR, WS_XC = WS_XB + 32 * XB_STR, WS_W1F = WS_XC + 32 * XC_STR;
constexpr int WS_W2F = WS_W1F + D * F * 2, WS_END = WS_W2F + D * F * 2;
constexpr int WS_STAMP = 32 << 20;
constexpr int NSTAMP = 24;
constexpr int UV_ROW = 544, UV_CH = 63 * UV_ROW;
constexpr int LD_UV = 0;
constexpr int LD_RED = 0;
constexpr int LD_AF = 0;
constexpr int LD_PART = 0;
constexpr int KC_CP = 4160, KC_CH = 2 * KC_CP;
constexpr int LD_KC = 68608;
constexpr int LD_WT = LD_KC + 16640;
constexpr int LD_XT = LD_WT + 16384;
constexpr int LD_MRED = LD_XT + 9216;
constexpr int LD_STATP = LD_MRED + 2560;
constexpr int LD_W7 = LD_KC;
constexpr int LD_STATS = 131072;
constexpr int LD_ZERO = LD_STATS + 1024;
constexpr int LD_HB = LD_ZERO + 512;
constexpr int LD_XBUF = LD_HB + 9216;
constexpr int LD_XRES = LD_XBUF + 8192;
constexpr int LD_VEC = LD_XRES + 8192;
constexpr int LD_CNT = LD_VEC + 2048 + 128;
constexpr int LD_CN2 = LD_CNT + 16;
constexpr int LD_END = LD_CN2 + 16;
static_assert(LD_W7 + 28672 <= LD_STATP && LD_STATP + 4096 <= 131072 && LD_END <= 163840, "LDS");
constexpr int AUX_SC1 = 16;
constexpr unsigned SENT = 0xFFFFFFFFu;
constexpr int OOB = 0x40000000;
constexpr int SPIN_CAP = 40000;
#define KNOB(name, val) constexpr int name = val
#define KB0 1
#define KSL 0
#define CDV 2
#define KSP 8
#define KQR 60
#define KH0 6
#define KW2A 0
#define KW2B 6
#define KROT4 0
#define W2P 3
#define KC1 7
#define KC2 10
#define KHB 6
#define KROTH 1
#define FLV 1
#define KNW2 22
#define W1V 0
#define KNWB 0
#define KNWD 2
#define KWE 70
#define KSW2 60
#define KLFF 56
#define KLDM 70
#define KLB 8
#define KGO 270
#define KTAIL 24
#define KQ2 2
#define KQE 60
#define KW2IN 28
#define KHW 8
#define KE1 12
#define KTE 16

DEV float asf(unsigned v) { return __builtin_bit_cast(float, v); }
DEV unsigned asu(float v) { return __builtin_bit_cast(unsigned, v); }
DEV float lo2f(unsigned v) { return asf(v << 16); }
DEV float hi2f(unsigned v) { return asf(v & 0xffff0000u); }
DEV unsigned pk(float a, float b) { bf2 v = {(__bf16)a, (__bf16)b}; return __builtin_bit_cast(unsigned, v); }
DEV unsigned pk(f2 a) { return pk(a.x, a.y); }
DEV float rbf(float a) { return lo2f(pk(a, a)); }
DEV f4 mfma(u4 a, u4 b, f4 c) { return __builtin_amdgcn_mfma_f32_16x16x32_bf16(__builtin_bit_cast(bf8, a), __builtin_bit_cast(bf8, b), c, 0, 0, 0); }
DEV void mfmaa(f4& c, const u4& a, const u4& b) { asm volatile("v_mfma_f32_16x16x32_bf16 %0, %1, %2, %0" : "+v"(c) : "v"(a), "v"(b)); }
DEV void mfma0(f4& c, const u4& a, const u4& b) { asm volatile("v_mfma_f32_16x16x32_bf16 %0, %1, %2, 0" : "=&v"(c) : "v"(a), "v"(b)); }
DEV void mfmaA(f16v& c, const u4& a, const u4& b) { asm volatile("v_mfma_f32_32x32x16_bf16 %0, %1, %2, %0" : "+v"(c) : "v"(a), "v"(b)); }
DEV void mfmaZ(f16v& c, const u4& a, const u4& b) { asm volatile("v_mfma_f32_32x32x16_bf16 %0, %1, %2, 0" : "=&v"(c) : "v"(a), "v"(b)); }
DEV void mfma_done12() { __builtin_amdgcn_sched_barrier(0); asm volatile("s_nop 11" ::: "memory"); __builtin_amdgcn_sched_barrier(0); }
DEV void mfma_done() { __builtin_amdgcn_sched_barrier(0); asm volatile("s_nop 7" ::: "memory"); __builtin_amdgcn_sched_barrier(0); }
constexpr int tri_k(int n) { int k = 0; while ((k + 1) * (16 - k) / 2 <= n) ++k; return k; }
template <int CTRL, int RM = 0xf> DEV float dpp0(float x) { return asf((unsigned)__builtin_amdgcn_update_dpp(0, (int)asu(x), CTRL, RM, 0xf, true)); }
DEV float sum8(float x) { x += dpp0<0xB1>(x); x += dpp0<0x4E>(x); return x + dpp0<0x141>(x); }
DEV float swap32_add(float a, float b) { auto r = __builtin_amdgcn_permlane32_swap(asu(a), asu(b), false, false); return asf(r[0]) + asf(r[1]); }
DEV float swap16_add(float a, float b) { auto r = __builtin_amdgcn_permlane16_swap(asu(a), asu(b), false, false); return asf(r[0]) + asf(r[1]); }
DEV float dot2(unsigned a, unsigned b, float c) { return __builtin_amdgcn_fdot2_f32_bf16(__builtin_bit_cast(bf2, a), __builtin_bit_cast(bf2, b), c, false); }
DEV void sb0() { __builtin_amdgcn_sched_barrier(0); }
DEV void bar() { sb0(); asm volatile("s_waitcnt lgkmcnt(0)\n\ts_barrier" ::: "memory"); sb0(); }
DEV void drain() { asm volatile("s_waitcnt vmcnt(0)" ::: "memory"); }
template <int N> DEV void waitvm() { asm volatile("s_waitcnt vmcnt(%0)" :: "n"(N) : "memory"); }
DEV void pin() { asm volatile("" ::: "memory"); sb0(); }
template <class T> DEV void keep(T& v) { asm volatile("" : "+v"(v)); }
template <int I, int N, class Fn> DEV void sfor(Fn&& f) { if constexpr (I < N) { f(std::integral_constant<int, I>{}); sfor<I + 1, N>(f); } }
DEV int opq(int v) { asm volatile("" : "+s"(v)); return v; }
DEV bool any_bad(bool bad) { return __builtin_amdgcn_ballot_w64(bad) != 0ull; }
DEV rsrc_t mkrs(const void* p, int bytes) { return __builtin_amdgcn_make_buffer_rsrc((void*)p, 0, bytes, 0x00020000); }
DEV u4 ld4(rsrc_t r, int vo, int so) { return __builtin_amdgcn_raw_buffer_load_b128(r, vo, so, (EXP & 1) ? 2 : 0); }
DEV u4 ld4c(rsrc_t r, int vo, int so) { return __builtin_amdgcn_raw_buffer_load_b128(r, vo, so, AUX_SC1); }
DEV u2 ld2c(rsrc_t r, int vo, int so) { return __builtin_amdgcn_raw_buffer_load_b64(r, vo, so, AUX_SC1); }
DEV void st4c(u4 v, rsrc_t r, int vo, int so) { asm("" : "+v"(v)); __builtin_amdgcn_raw_buffer_store_b128(v, r, vo, so, AUX_SC1); asm volatile("s_nop 1" :: "v"(v) : "memory"); }
DEV void st2c(u2 v, rsrc_t r, int vo, int so) { __builtin_amdgcn_raw_buffer_store_b64(v, r, vo, so, AUX_SC1); }
template <class T> DEV T gld(const void* base, unsigned off) { return *(const GAS T*)((const GAS unsigned char*)base + off); }
template <class T> DEV T gldn(const void* base, unsigned off) { if (EXP & 1) return __builtin_nontemporal_load((const GAS T*)((const GAS unsigned char*)base + off)); return gld<T>(base, off); }
DEV unsigned sld(const void* base, unsigned off) { return *(const CAS unsigned*)((const CAS unsigned char*)base + off); }
DEV unsigned umax3(unsigned a, unsigned b, unsigned c) { return max(max(a, b), c); }
DEV u4 trfrag(LAS unsigned char* a, int step) {
    const u2 t0 = __builtin_bit_cast(u2, __builtin_amdgcn_ds_read_tr16_b64_v4i16((LAS s4*)a)), t1 = __builtin_bit_cast(u2, __builtin_amdgcn_ds_read_tr16_b64_v4i16((LAS s4*)(a + step)));
    return u4{t0.x, t0.y, t1.x, t1.y};
}
DEV unsigned lds_ld(LAS unsigned char* a) { return __hip_atomic_load((LAS unsigned*)a, __ATOMIC_RELAXED, __HIP_MEMORY_SCOPE_WORKGROUP); }
DEV void lds_wait(LAS unsigned char* a, unsigned want) {
    while ((unsigned)__builtin_amdgcn_readfirstlane(lds_ld(a)) < want) __builtin_amdgcn_s_sleep(1);
    pin();
}
DEV unsigned lds_arrive(LAS unsigned char* a, int lane) {
    unsigned r = 0;
    pin();
    if (lane == 0) r = __hip_atomic_fetch_add((LAS unsigned*)a, 1u, __ATOMIC_RELAXED, __HIP_MEMORY_SCOPE_WORKGROUP);
    return __builtin_amdgcn_readfirstlane(r);
}
#define CM_MUL(t, a, b) "v_pk_mul_f32 " t ", " a ", " b " op_sel:[1,1] op_sel_hi:[1,0]\n\t"
#define CM_FMA(d, a, b) "v_pk_fma_f32 " d ", " a ", " b ", " d " op_sel_hi:[0,1,1] neg_lo:[0,0,1]\n\t"
DEV f2 cmul(f2 a, f2 b) { f2 d; asm(CM_MUL("%0", "%1", "%2") "s_nop 0\n\t" CM_FMA("%0", "%1", "%2") : "=&v"(d) : "v"(a), "v"(b)); return d; }
DEV void cmul2(f2& d0, f2 a0, f2 b0, f2& d1, f2 a1, f2 b1) {
    asm(CM_MUL("%0", "%2", "%3") CM_MUL("%1", "%4", "%5") CM_FMA("%0", "%2", "%3") CM_FMA("%1", "%4", "%5") : "=&v"(d0), "=&v"(d1) : "v"(a0), "v"(b0), "v"(a1), "v"(b1));
}
template <int O0, int O1, bool C0, bool C1> DEV void put2(int m0base, f2 a, f2 b) {
    unsigned t0, t1;
#define PUT2(N0, N1) asm volatile("s_mov_b32 m0, %2\n\tv_cvt_pk_bf16_f32 %0, %3, " N0 "%4\n\tv_cvt_pk_bf16_f32 %1, %5, " N1 "%6\n\tds_write_addtid_b32 %0 offset:%7\n\tds_write_addtid_b32 %1 offset:%8" \
        : "=&v"(t0), "=&v"(t1) : "s"(m0base), "v"(a.x), "v"(a.y), "v"(b.x), "v"(b.y), "n"(O0), "n"(O1) : "memory")
    if constexpr (!C0 && !C1) PUT2("", ""); else if constexpr (!C0) PUT2("", "-"); else PUT2("-", "-");
}
DEV int ssel(bool c, int a, int b) { asm("" : "+s"(a), "+s"(b)); return c ? a : b; }
template <class T> DEV void keeps(T& v) { asm volatile("" : "+s"(v)); }
DEV f2 zpow(float a2, float wr, float e) {
    const float m = __builtin_amdgcn_exp2f(a2 * e), ph = __builtin_amdgcn_fractf(wr * e);
    return f2{m * __builtin_amdgcn_cosf(ph), m * __builtin_amdgcn_sinf(ph)};
}
DEV int rnd(int r, int sh) { const int nz = sh > 0; return (r + ((1 << sh) >> 1) - nz + ((r >> sh) & nz)) >> sh; }
DEV u4 scale4(unsigned x0, unsigned x1, unsigned x2, unsigned x3, const unsigned (&sl)[4], const unsigned (&sh)[4]) {
    unsigned o0, o1, o2, o3, t0, t1, t2, t3;
    asm("v_dot2_f32_bf16 %0, %8, %12, 0\n\tv_dot2_f32_bf16 %4, %8, %16, 0\n\tv_dot2_f32_bf16 %1, %9, %13, 0\n\tv_dot2_f32_bf16 %5, %9, %17, 0\n\t"
        "v_dot2_f32_bf16 %2, %10, %14, 0\n\tv_dot2_f32_bf16 %6, %10, %18, 0\n\tv_dot2_f32_bf16 %3, %11, %15, 0\n\tv_dot2_f32_bf16 %7, %11, %19, 0\n\t"
        "v_cvt_pk_bf16_f32 %0, %0, %4\n\tv_cvt_pk_bf16_f32 %1, %1, %5\n\tv_cvt_pk_bf16_f32 %2, %2, %6\n\tv_cvt_pk_bf16_f32 %3, %3, %7"
        : "=&v"(o0), "=&v"(o1), "=&v"(o2), "=&v"(o3), "=&v"(t0), "=&v"(t1), "=&v"(t2), "=&v"(t3)
        : "v"(x0), "v"(x1), "v"(x2), "v"(x3), "v"(sl[0]), "v"(sl[1]), "v"(sl[2]), "v"(sl[3]), "v"(sh[0]), "v"(sh[1]), "v"(sh[2]), "v"(sh[3]));
    return u4{o0, o1, o2, o3};
}

struct Params {
    const u16 *x, *Are, *Aim, *Bre, *Bim, *Cre, *Cim, *logdt, *dskip, *w1, *b1, *w2, *b2, *c, *wmod;
    u16* out;
    unsigned char* ws;
};

#define STAMP(i) do { } while (0)
#define COUNT(id)
#define RETRY(id, BAD, RELOAD) do { if (__builtin_expect(any_bad(BAD), 0)) { int spin_ = 0; do { asm volatile("; retry " #id ::: "memory"); __builtin_amdgcn_s_sleep(2); RELOAD; COUNT(id); } while (any_bad(BAD) && ++spin_ < SPIN_CAP); } } while (0)

__global__ void __launch_bounds__(512, 2) fused(Params P) {
    extern __shared__ __attribute__((aligned(16))) unsigned char smem_[];
    LAS unsigned char* const sm = (LAS unsigned char*)smem_;
    STAMP(0);
    __builtin_amdgcn_s_setprio(3);
    const int tid = threadIdx.x, lane = tid & 63, w = __builtin_amdgcn_readfirstlane(tid >> 6);
    const int l15 = lane & 15, lg = lane >> 4;
    const int b = blockIdx.x, g8 = b & 7, idx = b >> 3, p = g8 * 32 + idx, cl = idx >> 3;
    const int pan1 = 8 * (g8 >> 1) + (idx >> 2), j1 = 4 * (g8 & 1) + (idx & 3);
    const int pan2 = p >> 3, j2 = p & 7;
    unsigned pfv = 0;
    if (w == 6) {
        unsigned long long pc; asm volatile("s_getpc_b64 %0" : "=s"(pc));
        const unsigned o = (unsigned)((lane & 7) * 32 + idx);
        pfv = gld<unsigned>((const void*)(pc & ~127ull), o < CODE_PF_LINES ? o * 128u : 0u);
    }
    const int kch = (w >> 1) & 1, wq = w & 3;
    const bool ismod = b >= 64, lo = w < 4;
    const int mcb = max(b - 64, 0) >> 2, mq = b & 3, mr = 128 * mq + 32 * wq + 4 * (lane >> 3);
    const int wB = 4 * b + wq, wS = (wB * 1171) >> 16, wF = wB - 56 * wS;
    int l16 = lane * 16;
    unsigned ko = (unsigned)(p * 512 + wq * 128 + lane * 2), xg = (unsigned)(p * 8192 + (lane >> 3) * 1024 + w * 128 + (lane & 7) * 16), co = mr * 2;
    const int c0 = (128 * mq + 32 * wq) * (6 * D * 2) + 128 * mcb;
    int s0_ = ssel(lo, wS * (64 * F) + 64 * wF, c0), d0_ = lo ? 16 * F * 2 : 6 * D * 2;
    int s1_ = ssel(lo, (wB >> 4) * (64 * D) + 64 * (wB & 15), c0 + 2 * 6 * D * 2), d1_ = lo ? 16 * D * 2 : 6 * D * 2;
    int nrec = ssel(lo, D * F * 2, ismod ? 6 * D * D * 2 : 0);
    int p0 = lo ? F * 2 : 4 * 6 * D * 2, p1 = lo ? D * 2 : 4 * 6 * D * 2, sh_ = lo ? 2 : 3, mk_ = lo ? 48 : 112;
    keeps(p0); keeps(p1); keeps(sh_); keeps(mk_);
    int v0 = __mul24(lane >> sh_, p0) + (l16 & mk_), v1 = __mul24(lane >> sh_, p1) + (l16 & mk_);
    keep(v0); keep(v1);
    keep(l16); keep(ko); keep(xg); keep(co); keeps(s0_); keeps(d0_); keeps(s1_); keeps(d1_); keeps(nrec);
    pin();
    asm volatile("" :: "s"(P.x), "s"(P.Are), "s"(P.Aim), "s"(P.Bre), "s"(P.Bim), "s"(P.Cre), "s"(P.Cim), "s"(P.logdt), "s"(P.w1), "s"(P.c), "s"(P.wmod), "s"(P.ws));

    const unsigned rAre = gld<u16>(P.Are, ko), rAim = gld<u16>(P.Aim, ko);
    const unsigned rBre = gld<u16>(P.Bre, ko), rBim = gld<u16>(P.Bim, ko), rCre = gld<u16>(P.Cre, ko), rCim = gld<u16>(P.Cim, ko);
    const u4 xblk = gld<u4>(P.x, xg);
    const u2 rc = gld<u2>(P.c, co);
    const unsigned ep = sld(P.ws, WS_EP), rdt2 = sld(P.logdt, 4 * p), rds = sld(P.dskip, 4 * p);
    pin();
    __builtin_amdgcn_s_setprio(0);
    const rsrc_t rw = mkrs(P.ws, WS_END);
    int one0 = lane == 0 ? 0 : OOB;
    int ta = LD_UV + kch * UV_CH + (w & 1) * 256; keeps(ta);
    int wtw = LD_WT + w * 2048 + l16, wtr = LD_WT + w * 2048 + (8 * (lg >> 1) + (l15 >> 2)) * 64 + (lg & 1) * 32 + (lane & 1) * 16 + (lane & 2) * 4;
    keep(wtw); keep(wtr);
    const int uvb = LD_UV + (w >> 2) * UV_CH + lg * 16;
    int ur0 = uvb + ((0x1E191000 >> (8 * wq)) & 0xff) * UV_ROW + __mul24(rnd(15 - l15, (0x2210 >> (4 * wq)) & 3), UV_ROW), ur1 = uvb + (35 + 3 * wq) * UV_ROW + __mul24(rnd(15 - l15, 3), UV_ROW);
    int va = uvb + 47 * UV_ROW + __mul24(l15, UV_ROW);
    int xtw = LD_XT + w * 1152 + (lane >> 3) * 144 + (lane & 7) * 16, xtr = LD_XT + w * 1152 + (lane & 3) * 288 + (lane >> 2) * 4;
    int stw = LD_MRED + 2048 + (lane >> 3) * 64 + w * 8, mrw = LD_MRED + wq * 256 + (8 * (l15 & 7) + 2 * lg) * 4;
    keep(one0); keep(ur0); keep(ur1); keep(va); keep(xtw); keep(xtr); keep(stw); keep(mrw);
    const int oobw = b < 224 ? 0 : OOB;
    int sW1 = WS_W1F + wF * 32768 + wS * 2048 + oobw, sF1 = WS_WFLAG + wF * 128 + wS * 8 + oobw, sXA = WS_XA + p * XA_STR + w * 2048;
    keeps(sW1); keeps(sF1); keeps(sXA);
    pin();
    if (KB0) { __builtin_amdgcn_s_barrier(); pin(); }
    if (KSL) __builtin_amdgcn_s_sleep(KSL);
    u4 wm[4];
    {
        const rsrc_t r0 = mkrs(lo ? P.w1 : P.wmod, nrec), r1 = mkrs(lo ? P.w2 : P.wmod, nrec);
        wm[0] = ld4(r0, v0, s0_); wm[1] = ld4(r0, v0, s0_ + d0_); wm[2] = ld4(r1, v1, s1_); wm[3] = ld4(r1, v1, s1_ + d1_);
    }
    pin();
    const unsigned tag = (unsigned)opq((int)(ep * 0x9E3779B1u + 0x7F4A7C15u));
    float dt;
    {
        const float x = asf((rdt2 << (kch ? 0 : 16)) & 0xffff0000u), t = x * 1.44269502f, c = __builtin_fmaf(x, 1.92596299e-8f, __builtin_fmaf(x, 1.44269502f, -t));
        const float e = __builtin_amdgcn_exp2f(t);
        dt = rbf(__builtin_fmaf(e, c * 0.693147181f, e));
    }
    f4 k0 = {0, 0, 0, (tid & 255) == 48 ? asf((rds << (w ? 0 : 16)) & 0xffff0000u) : 0.f};
    u2 flg = {~tag, tag};
    keep(dt); keep(k0); keep(flg);
    pin();
    STAMP(1);

    {
        const float a2 = (dt * lo2f(rAre)) * 1.44269504088896341f, wr = (dt * lo2f(rAim)) * 0.15915494309189535f;
        const f2 cf = cmul(f2{lo2f(rCre), lo2f(rCim)}, f2{dt * lo2f(rBre), dt * lo2f(rBim)});
        const f2 zz = zpow(a2, wr, w < 4 ? 1.f : 256.f);
#define UU(r0, a, r1, b) put2<(r0) * UV_ROW, (r1) * UV_ROW, false, false>(ta, a, b)
#define UV(r0, a, r1, b) put2<(r0) * UV_ROW, (r1) * UV_ROW, false, true>(ta, a, b)
#define VV(r0, a, r1, b) put2<(r0) * UV_ROW, (r1) * UV_ROW, true, true>(ta, a, b)
        if (w < 4) {
            f2 u[16], v[16], z2, z4, z8;
            u[0] = cf;
            cmul2(z2, zz, zz, u[1], cf, zz);
            cmul2(z4, z2, z2, u[2], u[0], z2); UU(0, u[0], 1, u[1]);
            cmul2(z8, z4, z4, u[3], u[1], z2);
            cmul2(v[1], z8, z8, u[4], u[0], z4); UU(2, u[2], 3, u[3]);
            cmul2(v[2], v[1], v[1], u[5], u[1], z4); UV(4, u[4], 48, v[1]);
            cmul2(v[4], v[2], v[2], u[6], u[2], z4); UV(5, u[5], 49, v[2]);
            cmul2(v[8], v[4], v[4], u[7], u[3], z4); UV(6, u[6], 51, v[4]);
            cmul2(v[3], v[1], v[2], u[8], u[0], z8); UV(7, u[7], 55, v[8]);
            cmul2(v[5], v[1], v[4], u[9], u[1], z8); UV(8, u[8], 50, v[3]);
            cmul2(v[6], v[2], v[4], u[10], u[2], z8); UV(9, u[9], 52, v[5]);
            cmul2(v[7], v[3], v[4], u[11], u[3], z8); UV(10, u[10], 53, v[6]);
            cmul2(v[9], v[1], v[8], u[12], u[4], z8); UV(11, u[11], 54, v[7]);
            cmul2(v[10], v[2], v[8], u[13], u[5], z8); UV(12, u[12], 56, v[9]);
            cmul2(v[11], v[3], v[8], u[14], u[6], z8); UV(13, u[13], 57, v[10]);
            cmul2(v[12], v[4], v[8], u[15], u[7], z8); UV(14, u[14], 58, v[11]);
            cmul2(v[13], v[5], v[8], v[14], v[6], v[8]); UV(15, u[15], 59, v[12]);
            v[15] = cmul(v[7], v[8]); VV(60, v[13], 61, v[14]);
            UV(47, (f2{1.f, 0.f}), 62, v[15]);
        } else {
            const f2 z2 = zpow(a2, wr, 2.f);
            f2 e[9], c2[5], c3[5], h[12], z4, z8, s2, s4;
            cmul2(e[0], cf, zz, s2, zz, zz);
            cmul2(z4, z2, z2, e[1], e[0], z2);
            cmul2(z8, z4, z4, e[2], e[0], z4); UU(16, e[0], 17, e[1]);
            cmul2(s4, s2, s2, e[3], e[1], z4);
            cmul2(e[4], e[0], z8, e[5], e[1], z8); UU(18, e[2], 19, e[3]);
            cmul2(e[6], e[2], z8, e[7], e[3], z8); UU(20, e[4], 21, e[5]);
            cmul2(e[8], e[4], z8, c2[0], e[0], zz); UU(22, e[6], 23, e[7]);
            cmul2(c2[1], e[2], zz, c2[2], e[4], zz); UU(24, e[8], 25, c2[0]);
            cmul2(c2[3], e[6], zz, c2[4], e[8], zz); UU(26, c2[1], 27, c2[2]);
            cmul2(c3[0], e[0], s2, c3[1], e[2], s2); UU(28, c2[3], 29, c2[4]);
            cmul2(c3[2], e[4], s2, c3[3], e[6], s2); UU(30, c3[0], 31, c3[1]);
            cmul2(c3[4], e[8], s2, h[0], c2[0], s2); UU(32, c3[2], 33, c3[3]);
            cmul2(h[1], c2[2], s2, h[2], c2[4], s2); UU(34, c3[4], 35, h[0]);
            cmul2(h[3], e[0], s4, h[4], e[4], s4); UU(36, h[1], 37, h[2]);
            cmul2(h[5], e[8], s4, h[6], c2[0], s4); UU(38, h[3], 39, h[4]);
            cmul2(h[7], c2[2], s4, h[8], c2[4], s4); UU(40, h[5], 41, h[6]);
            cmul2(h[9], c3[0], s4, h[10], c3[2], s4); UU(42, h[7], 43, h[8]);
            h[11] = cmul(c3[4], s4); UU(44, h[9], 45, h[10]); UU(46, h[11], 46, h[11]);
        }
    }
    pin();
    STAMP(2);

    auto stW2 = [&]() {
        *(LAS u4*)(sm + wtw) = wm[2]; *(LAS u4*)(sm + wtw + 1024) = wm[3];
        LAS unsigned char* a = sm + LD_WT + w * 2048 + (8 * lg + (l15 >> 2)) * 64 + (lane & 3) * 16;
        const u4 f0 = trfrag(a, 256), f1 = trfrag(a + 8, 256);
        const int sW2 = WS_W2F + ((wB & 15) >> 1) * 229376 + (wB >> 4) * 4096 + (wB & 1) * 2048 + oobw;
        st4c(f0, rw, l16, sW2); st4c(f1, rw, l16 + 1024, sW2);
    };
    if (opq(w) < 4) {
        *(LAS u4*)(sm + wtw) = wm[0]; *(LAS u4*)(sm + wtw + 1024) = wm[1];
        wm[0] = trfrag(sm + wtr, 256); wm[1] = trfrag(sm + wtr + 1024, 256);
        if (W1V == 0) { st4c(wm[0], rw, l16, sW1); st4c(wm[1], rw, l16 + 1024, sW1); }
    }
    pin();
    u4 g[2];
    auto stX = [&]() { st4c(g[0], rw, l16, sXA); st4c(g[1], rw, l16 + 1024, sXA); };
    {
        *(LAS u4*)(sm + xtw) = xblk;
#pragma unroll
        for (int i = 0; i < 2; ++i) g[i] = u4{*(LAS unsigned*)(sm + xtr + 64 * i), tag, *(LAS unsigned*)(sm + xtr + 144 + 64 * i), tag};
        float s = 0.f, q = 0.f;
#pragma unroll
        for (int i = 0; i < 4; ++i) { s = dot2(xblk[i], 0x3f803f80u, s); q = dot2(xblk[i], xblk[i], q); }
        s = sum8(s); keep(s); q = sum8(q); keep(q);
        if (XAV == 0) stX(); else { keep(g[0]); keep(g[1]); }
        *(LAS f2*)(sm + stw) = f2{s, q};
    }
    pin();
    if (opq(w) >= 4) {
        float sc[4] = {lo2f(rc.x), hi2f(rc.x), lo2f(rc.y), hi2f(rc.y)};
#pragma unroll
        for (int i = 0; i < 4; ++i) sc[i] *= __builtin_amdgcn_rcpf(1.f + __expf(-sc[i]));
        const unsigned sp0 = pk(sc[0], sc[1]), sp1 = pk(sc[2], sc[3]);
        float a[8];
#pragma unroll
        for (int i = 0; i < 4; ++i) {
            a[2 * i] = dot2(__builtin_amdgcn_perm(wm[3][i], wm[2][i], 0x05040100u), sp1, dot2(__builtin_amdgcn_perm(wm[1][i], wm[0][i], 0x05040100u), sp0, 0.f));
            a[2 * i + 1] = dot2(__builtin_amdgcn_perm(wm[3][i], wm[2][i], 0x07060302u), sp1, dot2(__builtin_amdgcn_perm(wm[1][i], wm[0][i], 0x07060302u), sp0, 0.f));
        }
        const float e0 = swap32_add(a[0], a[4]), e1 = swap32_add(a[1], a[5]), e2 = swap32_add(a[2], a[6]), e3 = swap32_add(a[3], a[7]);
        float g0 = swap16_add(e0, e2), g1_ = swap16_add(e1, e3);
        g0 += dpp0<0x128>(g0); g1_ += dpp0<0x128>(g1_);
        *(LAS f2*)(sm + mrw) = f2{g0, g1_};
    }
    if (opq(w) < 2) {
        const int a = tid < 96 ? LD_ZERO + (tid >> 6) * 4096 + tid * 16 : (tid < 112 ? LD_KC + ((tid >> 2) & 3) * KC_CP + 4080 + (tid & 3) * 16 : LD_CNT + (tid & 1) * 16);
        *(LAS u4*)(sm + a) = u4{0, 0, 0, 0};
    }
    if (FLV == 0 && opq(w) < 4) { waitvm<(XAV ? 0 : 2)>(); st2c(flg, rw, one0, sF1); }
    STAMP(3);
    bar();
    if (w == 2) {
        f2 s = {0.f, 0.f};
#pragma unroll
        for (int i = 0; i < 4; ++i) s += *(LAS f2*)(sm + LD_MRED + i * 256 + (lane & 31) * 8);
        st4c(u4{asu(s.x), tag, asu(s.y), tag}, rw, (ismod && lane < 32) ? lane * 64 : OOB, WS_MODG + mcb * 2048 + mq * 16);
    }
    if (w == 1) {
        const f2 sv = *(LAS f2*)(sm + LD_MRED + 2048 + lane * 8);
        const float m = sum8(sv.x) * (1.f / D), var = rbf(fmaxf(sum8(sv.y) * (1.f / D) - m * m, 0.f));
        st2c(u2{pk(m, __builtin_amdgcn_sqrtf(rbf(var + 1e-5f))), tag}, rw, (lane & 7) == 0 ? lane : OOB, WS_ST + p * 64);
    }
    pin();
    if (W1V == 1 && opq(w) < 4) { st4c(wm[0], rw, l16, sW1); st4c(wm[1], rw, l16 + 1024, sW1); }
    if (XAV == 1) stX();
    pin();
    int kcw, ab, xo, fo, go, hw, xw, bb, g1[7], sXB, sB1;
    {
        u4 fu[2][8], fv[8];
        f4 k1;
        auto rd = [&](int s) { fv[s] = *(LAS u4*)(sm + va + s * 64); fu[0][s] = *(LAS u4*)(sm + ur0 + s * 64); fu[1][s] = *(LAS u4*)(sm + ur1 + s * 64); };
#pragma unroll
        for (int s = 0; s < 4; ++s) rd(s);
        sb0();
        kcw = LD_KC + (w >> 2) * KC_CH + (2032 - 1024 * (lg & 1) - 256 * wq - 16 * l15 + 8 * (lg >> 1)) * 2;
        ab = LD_KC + (w >> 2) * KC_CH + (~l15 & 1) * KC_CP + ((15 - l15) >> 1) * 4 + (24 + lg - 8 * wq) * 16;
        xo = __mul24(lane >> 2, XA_STR) + (l16 & 48);
        { const int fa = w * 128 + l15 * 8, fb = min(l15 >> 1, 6) * 128 + ((14 + (l15 & 1) + 4 * cl) & 15) * 8; fo = tid < 448 ? fa : fb; }
        { const int ga = (760 + 8 * b) * 64 + l16, gb = (min(lane >> 2, 2) * 256 + p) * 64 + (lane & 3) * 16; go = lane >= (b < 96 ? 32 : 64) ? ga : gb; }
        hw = LD_HB + 512 + w * 512 + lane * 4; xw = LD_XBUF + w * 1024 + lane * 8;
        bb = LD_HB + (w >> 2) * 4608 + (208 - 64 * wq + 16 * l15 + 8 * lg) * 2;
        keep(kcw); keep(ab); keep(xo); keep(fo); keep(go); keep(hw); keep(xw); keep(bb);
        {
            int x = WS_W1F + (7 * j1 + (w < 7 ? w : 0)) * 32768, y = 8192 * cl + (w < 7 ? 0 : 0x7000), dx = w < 7 ? 0 : 32768, dy = w < 7 ? 4096 : 0;
            keeps(dx); keeps(dy);
#pragma unroll
            for (int g = 0; g < 7; ++g) { g1[g] = x + (y & 0x7000); keeps(g1[g]); x += dx; y += dy; keeps(x); keeps(y); }
        }
        pin();
        if (XAV == 2) { stX(); pin(); }
#pragma unroll
        for (int s = 0; s < 8; ++s) { if (s < 4) rd(s + 4); mfmaa(k0, fu[0][s], fv[s]); if (s == 0) mfma0(k1, fu[1][0], fv[0]); else mfmaa(k1, fu[1][s], fv[s]); sb0(); }
        mfma_done();
        const auto s0 = __builtin_amdgcn_permlane16_swap(pk(k0[0], k0[1]), pk(k1[0], k1[1]), false, false);
        const auto s1 = __builtin_amdgcn_permlane16_swap(pk(k0[2], k0[3]), pk(k1[2], k1[3]), false, false);
        const unsigned e01 = s0[0], e23 = s1[0], e45 = s0[1], e67 = s1[1];
        *(LAS u4*)(sm + kcw) = u4{e01, e23, e45, e67};
        *(LAS u16*)(sm + kcw + KC_CP - 2) = (u16)e01;
        *(LAS u3*)(sm + kcw + KC_CP) = u3{__builtin_amdgcn_alignbit(e23, e01, 16), __builtin_amdgcn_alignbit(e45, e23, 16), __builtin_amdgcn_alignbit(e67, e45, 16)};
        *(LAS u16*)(sm + kcw + KC_CP + 12) = (u16)(e67 >> 16);
    }
    pin();
    if (FLV == 1 && opq(w) < 4) { waitvm<2>(); st2c(flg, rw, one0, sF1); }
    STAMP(4);
    asm volatile("" :: "v"(pfv));
    u4 xa[2], st[2], mg; u2 fl;
    auto ldX = [&]() { xa[0] = ld4c(rw, xo, WS_XA + 32 * w * XA_STR + p * 64); xa[1] = ld4c(rw, xo, WS_XA + 32 * w * XA_STR + p * 64 + 16 * XA_STR); };
    auto ldS = [&]() { st[0] = ld4c(rw, l16, WS_ST + w * 2048); st[1] = ld4c(rw, l16 + 1024, WS_ST + w * 2048); mg = ld4c(rw, go, WS_MODG); };
    auto ldF = [&]() { fl = ld2c(rw, fo, WS_WFLAG + 7 * j1 * 128); };
    u4 wf[32];
    auto ldw1 = [&](int n) { wf[n] = ld4c(rw, l16 + (n & 3) * 1024, g1[n >> 2]); };
    constexpr int NWB = KNWB, NW0 = NWB + KNWD;
    if (LXV == 0) ldX();
    int ab1 = ab + 1024, ab2 = ab + 2048, ab3 = ab + 3072;
    keep(ab1); keep(ab2); keep(ab3);
    STAMP(5);
    bar();
    if (w == 7) {
#pragma unroll
        for (int i = 0; i < 8; ++i) st4c(u4{SENT, SENT, SENT, SENT}, rw, l16 + (i & 3) * 1024, WS_XB + (p >> 3) * XB_STR + (p & 7) * 8192 + (i >> 2) * 4096);
    }
    if (W2P == 3 && opq(w) < 4) stW2();
    if (LXV == 1) ldX();
    if (LXV < 2) ldS();
    u4 A[16];
    auto ldT = [&](int n) {
        const int o = (228 - 4 * ((n >> 3) + 8 * (n & 7))) * 16;
        LAS unsigned* ap = (LAS unsigned*)(sm + ((o >> 10) == 0 ? ab : ((o >> 10) == 1 ? ab1 : ((o >> 10) == 2 ? ab2 : ab3))) + (o & 1023));
        A[n] = u4{ap[0], ap[1], ap[2], ap[3]};
    };
#pragma unroll
    for (int n = 0; n < KTE; ++n) ldT(n);
    pin();
    if (LXV == 2) { ldX(); ldS(); }
    pin();
    STAMP(6);
    RETRY(0, ((xa[0].y ^ tag) | (xa[0].w ^ tag) | (xa[1].y ^ tag) | (xa[1].w ^ tag)) != 0, ldX());
    *(LAS u2*)(sm + xw) = u2{xa[0].x, xa[0].z}; *(LAS u2*)(sm + xw + 512) = u2{xa[1].x, xa[1].z};
    f4 x0 = {lo2f(xa[0].x), lo2f(xa[0].z), lo2f(xa[1].x), lo2f(xa[1].z)}, x1 = {hi2f(xa[0].x), hi2f(xa[0].z), hi2f(xa[1].x), hi2f(xa[1].z)};
    keep(x0); keep(x1);
    STAMP(7);
    RETRY(1, ((st[0].y ^ tag) | (st[0].w ^ tag) | (st[1].y ^ tag) | (st[1].w ^ tag) | (mg.y ^ tag) | (mg.w ^ tag)) != 0, { __builtin_amdgcn_s_sleep(4); ldS(); });
    ldF();
        pin();
        if (opq(w) < 4) st2c(flg, rw, one0, WS_WFLAG + 8192 + ((wB & 15) >> 1) * 896 + (wB >> 4) * 16 + (wB & 1) * 8 + oobw);
        if (w == 7) st2c(u2{(unsigned)__builtin_amdgcn_s_memrealtime(), tag}, rw, one0, WS_F2 + p * 8);
    STAMP(8);
    unsigned gap;
    {
        float m0_ = asf(mg.x), m1_ = asf(mg.z);
        m0_ += dpp0<0xB1>(m0_); keep(m0_); m1_ += dpp0<0xB1>(m1_); keep(m1_);
        const f4 m = {lo2f(st[0].x), lo2f(st[0].z), lo2f(st[1].x), lo2f(st[1].z)};
        m0_ += dpp0<0x4E>(m0_); keep(m0_); m1_ += dpp0<0x4E>(m1_); keep(m1_);
        const f4 r = {__builtin_amdgcn_rcpf(hi2f(st[0].x)), __builtin_amdgcn_rcpf(hi2f(st[0].z)), __builtin_amdgcn_rcpf(hi2f(st[1].x)), __builtin_amdgcn_rcpf(hi2f(st[1].z))};
        const unsigned mp = pk(m0_, m1_), m1p = pk(1.f + lo2f(mp), 1.f + hi2f(mp));
        const unsigned sap = __builtin_amdgcn_readlane(mp, 0), cap = __builtin_amdgcn_readlane(m1p, 4);
        gap = __builtin_amdgcn_readlane(mp, 8);
        const f4 h0 = ((x0 - m) * r) * lo2f(cap) + lo2f(sap), h1 = ((x1 - m) * r) * hi2f(cap) + hi2f(sap);
        *(LAS unsigned*)(sm + hw) = pk(h0[0], h0[1]); *(LAS unsigned*)(sm + hw + 256) = pk(h0[2], h0[3]);
        *(LAS unsigned*)(sm + hw + 4608) = pk(h1[0], h1[1]); *(LAS unsigned*)(sm + hw + 4608 + 256) = pk(h1[2], h1[3]);
        pin();
        if (w == 3) st2c(u2{(b >> 5) == 1 ? m1p : mp, tag}, rw, (lane >= 32 && (lane & 3) == 0 && b < 96) ? lane * 2 : OOB, WS_M2 + b * 64 - 64);
    }
    pin();
    bar();
    STAMP(9);

    u4 mv, xv; u2 f2v;
    int mo, vw, rdr, xso; unsigned rb1;
    auto ldM = [&]() { mv = ld4c(rw, mo, WS_M2); };
    auto ldF2 = [&]() { fl = ld2c(rw, min(lane, 13) * 8, WS_WFLAG + 8192 + j2 * 896 + w * 112); };
    u4 wg[28];
    int g2[7], gh[7];
    int rot7 = (KROT4 ? 2 * cl : 3 * (cl & 1)) + (KROTH == 1 ? 4 * ((j2 >> 1) & 1) : (KROTH == 2 ? 2 * (j2 >> 1) : 0));
    rot7 = rot7 >= 7 ? rot7 - 7 : rot7;
    auto base2 = [&](int i) {
        int k = i + rot7; k = k >= 7 ? k - 7 : k;
        g2[i] = WS_W2F + j2 * 229376 + (7 * w + k) * 4096; gh[i] = WS_XC + pan2 * XC_STR + (7 * w + k) * 4096; keeps(g2[i]); keeps(gh[i]);
    };
    auto ldw2 = [&](int n) { wg[n] = ld4c(rw, l16 + (n & 3) * 1024, g2[n >> 2]); };
    constexpr int W2A = KW2A, W2E = W2A + KW2B;
    auto ldFF = [&]() { f2v = ld2c(rw, lane * 8, WS_F2 + w * 512); };
    {
        f4 acc[8];
        int rdw;
        u4 B[16];
#pragma unroll
        for (int k = 0; k < 8; ++k) B[k] = *(LAS u4*)(sm + bb + (256 * k + 32) * 2);
        pin();
        RETRY(7, ((fl.y ^ tag) | (~fl.x ^ tag)) != 0, { __builtin_amdgcn_s_sleep(2); ldF(); });
#pragma unroll
        for (int n = 0; n < NW0; ++n) ldw1(n);
        pin();
        sfor<0, 72>([&](auto mc) __attribute__((always_inline)) {
            constexpr int m = decltype(mc)::value, rr = m / 36, n = m % 36, k = tri_k(n), i = n - (k * (17 - k)) / 2;
            if constexpr (m < 8) mfma0(acc[i + k], A[i], B[k]); else mfmaa(acc[i + k], A[8 * rr + i], B[8 * rr + k]);
            if constexpr (rr == 0 && n % 4 == 0 && n / 4 < 8 && 8 + n / 4 >= KTE) ldT(8 + n / 4);
            if constexpr (m >= 16 && m < 48 && m % 4 == 1) B[8 + (m - 16) / 4] = *(LAS u4*)(sm + bb + (256 * ((m - 16) / 4)) * 2);
            if constexpr (m < KWE && m * (KNW2 - NW0) / KWE < (m + 1) * (KNW2 - NW0) / KWE) ldw1(NW0 + m * (KNW2 - NW0) / KWE);
            if constexpr (m == KSW2) {
                sb0();
                if (opq(w) < 4) ldFF();
            }
            if constexpr (m >= 20 && m < 34 && m % 2 == 0) base2((m - 20) / 2);
            if constexpr (m == 18) { sXB = WS_XB + 4 * w * XB_STR + p * 256; sB1 = WS_XB + pan1 * XB_STR; keeps(sXB); keeps(sB1); }
            if constexpr (m == 38) { rdw = LD_RED + w * 8192 + (4 * l15 + (lg ^ ((l15 >> 1) & 3))) * 16; keep(rdw); }
            if constexpr (m == 42) { rdr = LD_RED + w * 1024 + ((lane & ~3) + ((lane & 3) ^ ((lane >> 3) & 3))) * 16; keep(rdr); }
            if constexpr (m == 46) { xso = lg * XB_STR + l15 * 16; keep(xso); }
            if constexpr (m == 50) { mo = (l16 & ssel(w < 4, 1023, 255)) + ssel(w < 4, w * 1024, 4096 + j2 * 256); keep(mo); }
            if constexpr (m == 54) { vw = ((8 * tid - 256 * cl) & ssel(w < 4, 1023, 127)) + ssel(w < 4, LD_VEC + (w < 2 ? 1024 : 0), LD_VEC + 2048); keep(vw); }
            if constexpr (m == 58) xv = *(LAS u4*)(sm + LD_XBUF + tid * 16);
            if constexpr (m == 62) rb1 = gld<u16>(P.b1, (224 * j1 + 32 * min(w, 6) + (lane & 19) + ((lane & 4) << 1) + ((lane & 8) >> 1)) * 2);
            if constexpr (m == 64) { x0 = f4{lo2f(xv.x), lo2f(xv.y), lo2f(xv.z), lo2f(xv.w)}; keep(x0); }
            if constexpr (m == 67) { x1 = f4{hi2f(xv.x), hi2f(xv.y), hi2f(xv.z), hi2f(xv.w)}; keep(x1); }
            if constexpr (m == KLDM) { ldM(); ldF2(); }
            if constexpr (rr == 1 && i + k == 7 && k >= 1 && k <= 4) *(LAS f4*)(sm + rdw + (k - 1) * 1024) = acc[k - 1];
            if constexpr (m == 35) STAMP(10);
            sb0();
        });
        static_assert(KSW2 < KLDM && KLDM > 50, "order");
        *(LAS f4*)(sm + rdw + 4 * 1024) = acc[4];
        mfma_done();
        *(LAS f4*)(sm + rdw + 5 * 1024) = acc[5]; *(LAS f4*)(sm + rdw + 6 * 1024) = acc[6]; *(LAS f4*)(sm + rdw + 7 * 1024) = acc[7];
    }
    if (opq(w) < 4) {
        RETRY(10, f2v.y != tag, { __builtin_amdgcn_s_sleep(6); ldFF(); });
        asm volatile("ds_max_u32 %0, %1" :: "v"(LD_CNT + 12), "v"(f2v.x) : "memory");
    }
    STAMP(11);
    bar();
    unsigned tgo;
    {
        f4 y[8];
#pragma unroll
        for (int i = 0; i < 8; ++i) y[i] = *(LAS f4*)(sm + rdr + i * 8192);
        tgo = lds_ld(sm + LD_CNT + 12);
        sb0();
        f4 y0 = x0 + lo2f(gap) * ((y[0] + y[1]) + (y[2] + y[3])), y1 = x1 + hi2f(gap) * ((y[4] + y[5]) + (y[6] + y[7]));
        keep(y0); keep(y1);
        u4 o;
#pragma unroll
        for (int r = 0; r < 4; ++r) o[r] = min(pk(y0[r], y1[r]), SENT - 1u);
        st4c(o, rw, xso, sXB);
    }
    pin();
    STAMP(12);
    tgo = __builtin_amdgcn_readfirstlane(tgo) + KGO;
    int vo = (l15 >> 1) == 2 * (lg & 1) ? LD_VEC + (lane & 1) * 1024 + (lg >> 1) * 16 : LD_ZERO, xbo = (tid >> 4) * 2048 + l15 * 16;
    keep(vo); keep(xbo);
    int afo = LD_AF + (((tid >> 4) - 8 * cl) & 31) * 1024 + l15 * 16, spo = LD_STATP + w * 512 + l15 * 16;
    const unsigned one = 0x3f803f80u;
    u4 sel = {l15 == 0 ? one : 0u, l15 == 1 ? one : 0u, l15 == 2 ? one : 0u, l15 == 3 ? one : 0u};
    keep(afo); keep(spo); keep(sel);
    int svo = LD_VEC + (((tid & ~15) - 128 * cl) & 511) * 2; keep(svo);
    RETRY(2, ((mv.y ^ tag) | (mv.w ^ tag)) != 0, { __builtin_amdgcn_s_sleep(4); ldM(); });
    *(LAS u2*)(sm + vw) = u2{mv.x, mv.z};
    RETRY(8, ((fl.y ^ tag) | (~fl.x ^ tag)) != 0, ldF2());
    bar();
    STAMP(13);
    const u4 sv0 = *(LAS u4*)(sm + svo), sv1 = *(LAS u4*)(sm + svo + 16);
#pragma unroll
    for (int n = KNW2; n < 28; ++n) ldw1(n);
#pragma unroll
    for (int n = 0; n < W2A; ++n) ldw2(n);
    pin();
    u4 xb[8];
    auto ldB = [&]() {
#pragma unroll
        for (int i = 0; i < 8; ++i) xb[i] = ld4c(rw, xbo + i * 256, sB1);
    };
    auto badB = [&]() {
        unsigned mx = 0;
#pragma unroll
        for (int i = 0; i < 8; ++i) mx = umax3(umax3(mx, xb[i].x, xb[i].y), xb[i].z, xb[i].w);
        return mx == SENT;
    };
    auto gate = [&]() {
        sb0();
#pragma unroll 1
        for (int i = 0; i < 64 && (int)(tgo - (unsigned)__builtin_amdgcn_s_memrealtime()) > 0; ++i) __builtin_amdgcn_s_sleep(1);
        __builtin_amdgcn_s_setprio(2);
#pragma unroll
        for (int i = 0; i < 8; ++i) { xb[i] = ld4c(rw, xbo + i * 256, sB1); sb0(); if (i == 3) __builtin_amdgcn_s_setprio(0); }
    };
    f4 q0;
    if (opq(w) != 7) {
        u4 vb[8];
#pragma unroll
        for (int n = 0; n < 6; ++n) vb[n] = *(LAS u4*)(sm + vo + n * 32);
        sb0();
        sfor<0, 28>([&](auto nc) __attribute__((always_inline)) {
            constexpr int n = decltype(nc)::value;
            if constexpr (n + 6 < 28) vb[(n + 6) & 7] = *(LAS u4*)(sm + vo + (n + 6) * 32);
            if constexpr (n == 0) mfma0(q0, vb[0], wf[0]); else mfmaa(q0, vb[n & 7], wf[n]);
            sb0();
        });
    }
    if (opq(w) == 7) {
        int a = LD_W7 + l16; keep(a);
        sfor<0, 28>([&](auto nc) __attribute__((always_inline)) {
            constexpr int n = decltype(nc)::value;
            *(LAS u4*)(sm + a + n * 1024) = wf[n];
            sb0();
        });
    }
    pin();
    unsigned sl[2][4], sh[2][4];
    {
#pragma unroll
        for (int i = 0; i < 4; ++i) { sl[0][i] = sv0[i] & 0xffffu; sh[0][i] = sv0[i] & 0xffff0000u; sl[1][i] = sv1[i] & 0xffffu; sh[1][i] = sv1[i] & 0xffff0000u; keep(sl[0][i]); keep(sh[0][i]); keep(sl[1][i]); keep(sh[1][i]); }
    }
    pin();
    gate();
    STAMP(14);

    for (int spin = 0;; ++spin) {
        f4 sm4;
        float sq[4] = {0, 0, 0, 0};
#pragma unroll
        for (int h = 0; h < 2; ++h) {
#pragma unroll
            for (int i = 4 * h; i < 4 * h + 4; ++i) {
#pragma unroll
                for (int r = 0; r < 4; ++r) sq[r] = dot2(xb[i][r], xb[i][r], sq[r]);
                if (i == 0) mfma0(sm4, sel, xb[0]); else mfmaa(sm4, sel, xb[i]);
                sb0();
            }
#pragma unroll
            for (int r = 0; r < 4; r += 2) {
                const u4 a0 = scale4(xb[4 * h][r], xb[4 * h + 1][r], xb[4 * h + 2][r], xb[4 * h + 3][r], sl[h], sh[h]);
                const u4 a1 = scale4(xb[4 * h][r + 1], xb[4 * h + 1][r + 1], xb[4 * h + 2][r + 1], xb[4 * h + 3][r + 1], sl[h], sh[h]);
                *(LAS u4*)(sm + afo + (r >> 1) * 32768 + h * 512) = a0; *(LAS u4*)(sm + afo + (r >> 1) * 32768 + h * 512 + 256) = a1;
            }
            pin();
        }
        const float t = swap16_add(swap32_add(sq[0], sq[2]), swap32_add(sq[1], sq[3]));
        *(LAS float*)(sm + spo + 256 + lg * 4) = t;
        if (lg == 0) *(LAS f4*)(sm + spo) = sm4;
        if (__builtin_expect(!any_bad(t != t), 1)) break;
        if (!any_bad(badB()) || spin >= SPIN_CAP) break;
        COUNT(3);
        __builtin_amdgcn_s_sleep(4);
        ldB();
    }
#pragma unroll
    for (int n = W2A; n < W2E; ++n) ldw2(n);
    STAMP(15);
    bar();
    STAMP(16);
    auto raise = [&](int ev) {
        if (lds_arrive(sm + LD_CNT + 8 * ev, lane) == 6u) st2c(flg, rw, lane < 8 ? lane * 128 : OOB, WS_FLC + (pan1 * 8 + j1) * 1024 + ev * 64);
    };
    constexpr int W2IN = KW2IN;
    u4 o[2];
    int hso;
    const int sXC = WS_XC + pan1 * XC_STR + (7 * j1 + w) * 4096;
    auto sth = [&](int r, int h) { st4c(o[h], rw, hso + r * 2048 + h * 512, sXC); };
    u4 hb0[5];
#pragma unroll
    for (int q = 0; q < 5; ++q) { hb0[q] = *(LAS u4*)(sm + LD_AF + (q & 1) * 32768 + (q >> 1) * 1024 + l16); sb0(); }
    if (opq(w) != 7) {
        f16v ac[2];
        constexpr int T = KTAIL, NH = 64, NK = 2 * (32 - T), PF = 5, Q2 = KQ2, QE = KQE;
        constexpr auto fs = [](int q) constexpr { return q < NK ? q >> 1 : 32 - T + (q - NK) % T; };
        constexpr auto fr = [](int q) constexpr { return q < NK ? q & 1 : (q - NK) / T; };
        u4 hb[8], aq, bq[2], vl[4];
        float rs[2];
        int w7o;
        constexpr auto cum = [](int q) constexpr {
            int c = W2E;
            for (int i = Q2; i < q; ++i) {
                const int freed = i < NK ? i / 2 : (i < NK + T ? 32 - T : i - NK - 2 * T + 32), lin = W2E + (i + 1 - Q2) * (W2IN - W2E) / (QE - Q2);
                if (c < W2IN && c < lin && c < freed + KSP) ++c;
            }
            return c;
        };
        static_assert(cum(NH) == W2IN, "w2 schedule");
#pragma unroll
        for (int q = 0; q < PF; ++q) hb[q] = hb0[q];
        auto epi = [&](int r, int c) {
            f4 v = f4{ac[r][4 * c], ac[r][4 * c + 1], ac[r][4 * c + 2], ac[r][4 * c + 3]} * rs[r];
            v = __builtin_elementwise_max(v, f4{0, 0, 0, 0});
            v = v * v;
            keep(v);
            o[c >> 1][2 * (c & 1)] = pk(v[0], v[1]); o[c >> 1][2 * (c & 1) + 1] = pk(v[2], v[3]);
            if (c & 1) { if (r == 0) sth(0, c >> 1); else keep(o[c >> 1]); }
        };
        sfor<0, NH>([&](auto qc) __attribute__((always_inline)) {
            constexpr int q = decltype(qc)::value, s = fs(q), r = fr(q);
            if constexpr (q + PF < NH) hb[(q + PF) & 7] = *(LAS u4*)(sm + LD_AF + fr(q + PF) * 32768 + fs(q + PF) * 1024 + l16);
            if constexpr (q == 1) { w7o = LD_W7 + w * 4096 + l16; keep(w7o); }
            if constexpr (q >= 2 && q < 6) { wf[26 + q] = *(LAS u4*)(sm + w7o + (q - 2) * 1024); vl[q - 2] = *(LAS u4*)(sm + vo + (26 + q) * 32); }
            if constexpr (cum(q + 1) > cum(q)) { constexpr int n2 = cum(q); ldw2(n2); }
            if constexpr (s == 0) mfmaZ(ac[r], wf[0], hb[q & 7]); else mfmaA(ac[r], wf[s], hb[q & 7]);
            if constexpr (q >= 12 && q < 16) mfmaa(q0, vl[q - 12], wf[16 + q]);
            if constexpr (q == 20) {
                keep(q0);
                const float bs = lane < 32 ? q0[1] + lo2f(rb1) : 0.f;
                const unsigned hi = pk(q0[0], bs);
                const unsigned lo = pk(q0[0] - lo2f(hi), bs - hi2f(hi));
                aq = u4{__builtin_amdgcn_perm(lo, hi, 0x05040100u), __builtin_amdgcn_perm(lo, hi, 0x07060302u), 0u, 0u};
                keep(aq);
            }
            if constexpr (q == 22) {
                lds_wait(sm + LD_CNT + 4, 1u);
#pragma unroll
                for (int t = 0; t < 2; ++t) bq[t] = *(LAS u4*)(sm + LD_STATS + (l15 * 4 + (lg & 1)) * 16 + t * 32);
            }
            if constexpr (q == 26) { rs[0] = asf(bq[0].z); rs[1] = asf(bq[1].z); keep(rs[0]); keep(rs[1]); bq[0].z = 0u; bq[1].z = 0u; keep(bq[0]); keep(bq[1]); }
            if constexpr (q == 28 || q == 29) mfmaA(ac[q & 1], aq, bq[q & 1]);
            if constexpr (q == 30) { hso = (lg & 1) * 1024 + (lg >> 1) * 256 + l15 * 16; keep(hso); }
            sb0();
            if constexpr (q >= NK + T + 3 && q < NK + T + 7) epi(0, q - NK - T - 3);
            if constexpr (CDV == 2 && q == KQR) { waitvm<cum(q + 1) - cum(NK + T + 7)>(); raise(0); }
            sb0();
        });
        static_assert(NK + T > 30 && NK >= 6 && T >= 7 && (CDV != 2 || KQR > NK + T + 6), "schedule");
        STAMP(17);
        mfma_done12();
        epi(1, 0); epi(1, 1); epi(1, 2); epi(1, 3);
        pin();
        if (CDV == 1) { drain(); raise(0); sth(1, 0); sth(1, 1); }
        else lds_arrive(sm + LD_CN2, lane);
        STAMP(23);
    }
    if (opq(w) == 7) {
        auto ldR = [&]() {
#pragma unroll
            for (int i = 0; i < 8; ++i) xb[i] = ld4c(rw, lg * 2048 + l15 * 16 + i * 256, WS_XB + pan2 * XB_STR + j2 * 8192);
        };
        ldR();
        f2 t = {0.f, 0.f};
        int sp = LD_STATP + lane * 4; keep(sp);
#pragma unroll
        for (int i = 0; i < 8; ++i) t += f2{*(LAS float*)(sm + sp + i * 512), *(LAS float*)(sm + sp + i * 512 + 256)};
        const float m = t.x * (1.f / D), var = rbf(fmaxf(t.y * (1.f / D) - m * m, 0.f));
        const float sd = __builtin_amdgcn_sqrtf(rbf(var + 1e-5f));
        const unsigned sdp = pk(sd, sd);
        *(LAS u4*)(sm + LD_STATS + l16) = u4{pk(-m, -m), sdp, asu(__builtin_amdgcn_rcpf(lo2f(sdp))), 0u};
        lds_arrive(sm + LD_CNT + 4, lane);
        RETRY(6, badB(), ldR());
#pragma unroll
        for (int r = 0; r < 4; ++r) {
            LAS unsigned char* xr = sm + LD_XRES + (4 * l15 + r) * 128 + lg * 32;
            *(LAS u4*)xr = u4{xb[0][r], xb[1][r], xb[2][r], xb[3][r]}; *(LAS u4*)(xr + 16) = u4{xb[4][r], xb[5][r], xb[6][r], xb[7][r]};
        }
        lds_arrive(sm + LD_CNT + 4, lane);
        if (b == 0) __builtin_amdgcn_raw_buffer_store_b32(tag, rw, one0, WS_EP, AUX_SC1);
        pin();
#pragma unroll
        for (int n = W2E; n < W2IN; ++n) { ldw2(n); __builtin_amdgcn_s_sleep(2); sb0(); }
        STAMP(17);
    }
    pin();
    const u4 rb2 = gld<u4>(P.b2, (64 * j2 + 8 * (lane & 7)) * 2);
    if (CDV == 1) {
#pragma unroll
        for (int n = W2IN; n < 28; ++n) ldw2(n);
    }

    f4 g0, g1v, e0, e1;
    int ppr; unsigned oo;
    {
        u4 hm[28];
        int ho0 = l16 + (j2 & 1) * 1024, ho1 = ho0 ^ 1024;
        int pp0 = LD_PART + w * 16384 + lg * 256 + ((l15 + 4 * lg) & 15) * 16 + (j2 & 1) * 4096, pp1 = pp0 ^ 4096;
        keep(ho0); keep(ho1); keep(pp0); keep(pp1);
        auto ldh = [&](int n) { hm[n] = ld4c(rw, (((n / 7) & 1) ? ho1 : ho0) + (n / 14) * 2048, gh[n % 7]); };
        u2 fc;
        auto ldC = [&](int ev) { fc = ld2c(rw, 0, WS_FLC + (pan2 * 8 + w) * 1024 + j2 * 128 + ev * 64); };
        constexpr int HW = KHW;
        static_assert(HW <= 14 && KC2 + HW >= 14 && KC2 < 14 && KC1 < KC2, "window");
        if (CDV == 1) {
            lds_wait(sm + LD_CNT, 7u);
            ldC(0);
            drain();
            if (opq(w) != 7) raise(1);
            STAMP(18);
            RETRY(4, ((fc.y ^ tag) | (~fc.x ^ tag)) != 0, ldC(0));
            pin();
            STAMP(19);
#pragma unroll
            for (int n = 0; n < HW; ++n) { ldh(n); sb0(); }
        } else {
            lds_wait(sm + LD_CN2, 7u);
            ldC(0);
            sb0(); __builtin_amdgcn_s_barrier(); sb0();
            { const rsrc_t rs_ = mkrs(P.ws, w < 7 ? WS_END : 0); st4c(o[0], rs_, hso + 2048, sXC); st4c(o[1], rs_, hso + 2048 + 512, sXC); }
            STAMP(18);
            RETRY(4, ((fc.y ^ tag) | (~fc.x ^ tag)) != 0, ldC(0));
            pin();
            STAMP(19);
#pragma unroll
            for (int n = 0; n < KH0; ++n) { ldh(n); sb0(); }
            waitvm<KH0>();
            if (opq(w) != 7) raise(1);
#pragma unroll
            for (int n = W2IN; n < 28; ++n) ldw2(n);
#pragma unroll
            for (int n = KH0; n < HW; ++n) { ldh(n); sb0(); }
        }
        {
            lds_wait(sm + LD_CNT + 4, 2u);
            const int rt = w >> 1, tp = 8 * (w & 1) + (lane >> 3), ck = lane & 7, g = ck & 3, row = 4 * tp + rt;
            ppr = LD_PART + rt * 4096 + (ck >> 2) * 2048 + (g * 16 + ((tp + 4 * g) & 15)) * 16;
            oo = (unsigned)((64 * pan2 + row) * D + 64 * j2 + 8 * ck) * 2u;
            const u4 xr = *(LAS u4*)(sm + LD_XRES + row * 128 + ck * 16), gv = *(LAS u4*)(sm + LD_VEC + 2048 + ck * 16);
            g0 = f4{lo2f(gv.x), hi2f(gv.x), lo2f(gv.y), hi2f(gv.y)}; g1v = f4{lo2f(gv.z), hi2f(gv.z), lo2f(gv.w), hi2f(gv.w)};
            e0 = f4{lo2f(xr.x), hi2f(xr.x), lo2f(xr.y), hi2f(xr.y)} + g0 * f4{lo2f(rb2.x), hi2f(rb2.x), lo2f(rb2.y), hi2f(rb2.y)};
            e1 = f4{lo2f(xr.z), hi2f(xr.z), lo2f(xr.w), hi2f(xr.w)} + g1v * f4{lo2f(rb2.z), hi2f(rb2.z), lo2f(rb2.w), hi2f(rb2.w)};
            keep(ppr); keep(oo); keep(g0); keep(g1v); keep(e0); keep(e1);
            pin();
        }
        f4 ac[2][4];
        auto stp = [&](int rt, int m) { *(LAS f4*)(sm + ((rt & 1) ? pp1 : pp0) + (rt >> 1) * 8192 + m * 1024) = ac[rt & 1][m]; };
        sfor<0, 28>([&](auto nc) __attribute__((always_inline)) {
            constexpr int n = decltype(nc)::value, rt = n / 7, i = n % 7;
#pragma unroll
            for (int m = 0; m < 4; ++m) { if (i == 0) mfma0(ac[rt & 1][m], wg[m], hm[n]); else mfmaa(ac[rt & 1][m], wg[4 * i + m], hm[n]); }
            if constexpr (n + HW < 14) ldh(n + HW);
            if constexpr (n == KC1) ldC(1);
            if constexpr (n == KC2) {
                RETRY(11, ((fc.y ^ tag) | (~fc.x ^ tag)) != 0, ldC(1));
#pragma unroll
                for (int k = 14; k < 14 + KHB; ++k) { ldh(k); sb0(); }
            }
            if constexpr (n > KC2 && 13 + KHB + n - KC2 < 28) ldh(13 + KHB + n - KC2);
            if constexpr (rt > 0 && i >= 1 && i <= 4) stp(rt - 1, i - 1);
            sb0();
        });
        mfma_done();
        stp(3, 0); stp(3, 1); stp(3, 2); stp(3, 3);
    }
    STAMP(20);
    bar();
    {
        int ppr2 = ppr + 65536; keep(ppr2);
        f4 a[8], c[8];
#pragma unroll
        for (int i = 0; i < 8; ++i) { LAS unsigned char* q = sm + (i < 4 ? ppr : ppr2) + (i & 3) * 16384; a[i] = *(LAS f4*)q; c[i] = *(LAS f4*)(q + 1024); }
        sb0();
        f4 s0 = e0 + g0 * (((a[0] + a[1]) + (a[2] + a[3])) + ((a[4] + a[5]) + (a[6] + a[7]))), s1 = e1 + g1v * (((c[0] + c[1]) + (c[2] + c[3])) + ((c[4] + c[5]) + (c[6] + c[7])));
        keep(s0); keep(s1);
        *(GAS u4*)((GAS unsigned char*)P.out + oo) = u4{pk(s0[0], s0[1]), pk(s0[2], s0[3]), pk(s1[0], s1[1]), pk(s1[2], s1[3])};
    }
}

}

extern "C" void kernel_launch(void* const* d_in, const int* in_sizes, int n_in, void* d_out, int out_size, void* d_ws, size_t ws_size, hipStream_t stream) {
    using namespace mk;
    Params P;
    P.x = (const u16*)d_in[0]; P.Are = (const u16*)d_in[1]; P.Aim = (const u16*)d_in[2]; P.Bre = (const u16*)d_in[3]; P.Bim = (const u16*)d_in[4];
    P.Cre = (const u16*)d_in[5]; P.Cim = (const u16*)d_in[6]; P.logdt = (const u16*)d_in[7]; P.dskip = (const u16*)d_in[8];
    P.w1 = (const u16*)d_in[10]; P.b1 = (const u16*)d_in[11]; P.w2 = (const u16*)d_in[12]; P.b2 = (const u16*)d_in[13];
    P.c = (const u16*)d_in[14]; P.wmod = (const u16*)d_in[15];
    P.out = (u16*)d_out; P.ws = (unsigned char*)d_ws;
    (void)hipFuncSetAttribute((const void*)fused, hipFuncAttributeMaxDynamicSharedMemorySize, LD_END);
    hipLaunchKernelGGL(fused, dim3(256), dim3(512), LD_END, stream, P);
}
```
